```python
import torch

def _softmax(x, axis=-1):
    return torch.softmax(x, dim=axis)
def _gelu(x):
    return torch.nn.functional.gelu(x, approximate="tanh")
def _silu(x):
    return torch.nn.functional.silu(x)
def _sigmoid(x):
    return torch.sigmoid(x)
def _layernorm(x, axis=-1, eps=1e-5):
    m = x.mean(dim=axis, keepdim=True)
    v = x.var(dim=axis, keepdim=True, unbiased=False)
    return (x - m) / torch.sqrt(v + eps)
def _scatter_add(tgt, idx, src, axis=0):
    return tgt.clone().index_add_(axis, idx, src)
def _segment_sum(src, seg, n):
    out = torch.zeros((n,) + src.shape[1:], dtype=src.dtype)
    return out.index_add_(0, seg, src)
def _pad(x, pw):
    flat = tuple(v for pair in reversed(pw) for v in pair)
    return torch.nn.functional.pad(x, flat)

kernel_name = 'sequential_preLN__ssm_conv_s4d__dense_activation_gelu__prompt'

def setup_inputs():

    _w = (lambda a: (lambda t: t.to(torch.bfloat16) if t.is_floating_point() else t)(torch.as_tensor(a)))
    def _A_setup():
        Bsz, L, D, N = 1, 2048, 448, 256
        return {
            "u": _w((lambda *s: torch.randn(*s, dtype=torch.bfloat16) * 0.05)(Bsz, L, D)),
            "log_A_real": _w((lambda *s: torch.randn(*s, dtype=torch.bfloat16) * 6.4)(D, N) * 0.5),
            "A_imag": _w((lambda *s: torch.randn(*s, dtype=torch.bfloat16) * 0.05)(D, N) * 3.14),
            "C_real": _w((lambda *s: torch.randn(*s, dtype=torch.bfloat16) * 0.05)(D, N) / N ** 0.5),
            "C_imag": _w((lambda *s: torch.randn(*s, dtype=torch.bfloat16) * 0.05)(D, N) / N ** 0.5),
            "log_dt": _w((lambda *s: torch.randn(*s, dtype=torch.bfloat16) * 0.05)(D) * 0.5 - 4.0),
            "D_skip": _w((lambda *s: torch.randn(*s, dtype=torch.bfloat16) * 0.05)(D)),
        }
    _A_in = _A_setup()

    def _B_setup():
        B, S, D, F = 1, 2048, 448, 1792
        return {
            "x": _w((lambda *s: torch.randn(*s, dtype=torch.bfloat16) * 0.05)(B, S, D)),
            "w1": _w((lambda *s: torch.randn(*s, dtype=torch.bfloat16) * 0.8)(D, F) * (D ** -0.5)),
            "b1": _w((lambda *s: torch.randn(*s, dtype=torch.bfloat16) * 0.2)(F) * 0.01),
            "w2": _w((lambda *s: torch.randn(*s, dtype=torch.bfloat16) * 0.2)(F, D) * (F ** -0.5)),
            "b2": _w((lambda *s: torch.randn(*s, dtype=torch.bfloat16) * 0.2)(D) * 0.01),
        }
    _B_in = _B_setup()

    return {
        "A_u": _A_in["u"],
        "A_log_A_real": _A_in["log_A_real"],
        "A_A_imag": _A_in["A_imag"],
        "A_C_real": _A_in["C_real"],
        "A_C_imag": _A_in["C_imag"],
        "A_log_dt": _A_in["log_dt"],
        "A_D_skip": _A_in["D_skip"],
        "B_x": _B_in["x"],
        "B_w1": _B_in["w1"],
        "B_b1": _B_in["b1"],
        "B_w2": _B_in["w2"],
        "B_b2": _B_in["b2"],
    }

def reference(A_u, A_log_A_real, A_A_imag, A_C_real, A_C_imag, A_log_dt, A_D_skip, B_x, B_w1, B_b1, B_w2, B_b2):
    def _A_ref(u, log_A_real, A_imag, C_real, C_imag, log_dt, D_skip):
        L = u.shape[1]
        dt = torch.exp(log_dt)
        A = -torch.exp(log_A_real) + 1j * A_imag
        C = (C_real + 1j * C_imag) * dt[:, None]
        pos = (lambda x, dt: x.to(dt))(torch.arange(L), log_dt.dtype)
        P = torch.exp((A * dt[:, None])[:, :, None] * pos[None, None, :])
        K = 2.0 * torch.einsum("dn,dnl->dl", C, P).real
        L2 = 2 * L
        u_t = (lambda x, *d: x.permute(*d))(u, 0, 2, 1)
        Kf = (lambda x, *a, **k: torch.fft.rfft(x.float(), *a, **k))(K, L2)
        Uf = (lambda x, *a, **k: torch.fft.rfft(x.float(), *a, **k))(u_t, L2)
        y = (lambda x, *a, **k: torch.fft.irfft(x, *a, **k).to(torch.bfloat16))(Uf * Kf[None, :, :], L2)[:, :, :L]
        y = (lambda x, *d: x.permute(*d))(y, 0, 2, 1)
        return y + u * D_skip[None, None, :]

    def _B_ref(x, w1, b1, w2, b2):
        h = torch.einsum("bsd,df->bsf", x, w1) + b1
        h = _gelu(h)
        return torch.einsum("bsf,fd->bsd", h, w2) + b2

    x = A_u
    h = x + _A_ref(_layernorm(x, axis=-1), A_log_A_real, A_A_imag, A_C_real, A_C_imag, A_log_dt, A_D_skip)
    h2 = h + _B_ref(_layernorm(h, axis=-1), B_w1, B_b1, B_w2, B_b2)
    return h2
```

```cpp
#include <hip/hip_runtime.h>
#include <hip/hip_bf16.h>
#include <stdint.h>

#define LL 2048
#define DD 448
#define NN 256
#define FF 1792

typedef __bf16 bf16_t;
typedef __bf16 bf16x2 __attribute__((ext_vector_type(2)));
typedef __bf16 bf16x4 __attribute__((ext_vector_type(4)));
typedef __bf16 bf16x8 __attribute__((ext_vector_type(8)));
typedef short s16x4 __attribute__((ext_vector_type(4)));
typedef float f32x4 __attribute__((ext_vector_type(4)));
typedef float f32x16 __attribute__((ext_vector_type(16)));
typedef unsigned u32x4 __attribute__((ext_vector_type(4)));
typedef unsigned u32x2 __attribute__((ext_vector_type(2)));
typedef __attribute__((address_space(3))) s16x4* lds_s16x4_ptr;

#define DI __device__ __forceinline__

DI float bfr(float x) { return (float)(__bf16)x; }
DI float bf2f(unsigned short v) { return __uint_as_float(((unsigned)v) << 16); }
DI float bflo(unsigned w) { return __uint_as_float(w << 16); }
DI float bfhi(unsigned w) { return __uint_as_float(w & 0xffff0000u); }
DI unsigned pack2(float a, float b) {
  bf16x2 r = {(__bf16)a, (__bf16)b};
  return __builtin_bit_cast(unsigned, r);
}

#define WAIT_V(n) asm volatile("s_waitcnt vmcnt(" #n ")" ::: "memory")
#define WAIT_L(n) asm volatile("s_waitcnt lgkmcnt(" #n ")" ::: "memory")
#define WAIT_V_BAR(n) asm volatile("s_waitcnt vmcnt(" #n ") lgkmcnt(0)\n\ts_barrier" ::: "memory")

#define K1_KGEN_BLOCKS DD
#define K1_LN_BLOCKS 64
#define K1_CS_BLOCKS 14

DI void cexp_p(float lr2, float lih, float lil, float p, float& re, float& im) {
  float mag = __builtin_amdgcn_exp2f(lr2 * p);
  float ph = lih * p;
  float e = __builtin_fmaf(lih, p, -ph);
  float fr = __builtin_amdgcn_fractf(ph) + __builtin_fmaf(lil, p, e);
  re = mag * __builtin_amdgcn_cosf(fr);
  im = mag * __builtin_amdgcn_sinf(fr);
}

__global__ __launch_bounds__(512) void k1_kernel(
    const unsigned short* __restrict__ x, const unsigned short* __restrict__ logA,
    const unsigned short* __restrict__ Aim, const unsigned short* __restrict__ Cre,
    const unsigned short* __restrict__ Cim, const unsigned short* __restrict__ logdt,
    const unsigned short* __restrict__ w1, unsigned short* __restrict__ uT,
    unsigned short* __restrict__ xT, unsigned short* __restrict__ Kx, float* __restrict__ S1) {
  __shared__ __attribute__((aligned(16))) char smem[57344];
  const int tid = threadIdx.x, lane = tid & 63, wv = tid >> 6;
  const int bid = blockIdx.x;
  if (bid < K1_KGEN_BLOCKS) {
    const int d = bid;
    float* tab = (float*)smem;
    float* red = (float*)(smem + 8192);
    float* Kd = (float*)(smem + 8192 + 24576);
    if (tid < NN) {
      const int n = tid;
      float dt = bfr(expf(bf2f(logdt[d])));
      float Ar = -bfr(expf(bf2f(logA[d * NN + n])));
      float lr = Ar * dt;
      float li = bf2f(Aim[d * NN + n]) * dt;
      const float I2PH = 0.15915494f;
      const float I2PL = 6.4206382e-09f;
      float lih = li * I2PH;
      float lil = __builtin_fmaf(li, I2PH, -lih) + li * I2PL;
      f32x4 t0 = {lr * 1.44269504f, lih, lil, 0.f};
      f32x4 t1 = {2.f * bf2f(Cre[d * NN + n]) * dt, 2.f * bf2f(Cim[d * NN + n]) * dt, 0.f, 0.f};
      *(f32x4*)(tab + n * 8) = t0;
      *(f32x4*)(tab + n * 8 + 4) = t1;
    }
    __syncthreads();
    const int i = lane & 15, kq = lane >> 4;
    const float pA = (float)(8 * i);
    const float pB0 = (float)(128 * (i >> 3) + (i & 7));
    const float pB1 = (float)(i < 8 ? 256 + 128 * (i >> 2) + 2 * (i & 3) : 512 + 128 * ((i - 8) >> 1) + 4 * ((i - 8) & 1));
    const float pB2 = (float)(1024 + 128 * (i < 8 ? i : 8));
    f32x4 acc0 = {0, 0, 0, 0}, acc1 = {0, 0, 0, 0}, acc2 = {0, 0, 0, 0};
#pragma unroll 2
    for (int g = 0; g < 8; ++g) {
      const int n = 32 * wv + 4 * g + kq;
      f32x4 t0 = *(const f32x4*)(tab + n * 8);
      f32x4 t1 = *(const f32x4*)(tab + n * 8 + 4);
      float er, ei;
      cexp_p(t0[0], t0[1], t0[2], pA, er, ei);
      float ar = t1[0] * er - t1[1] * ei;
      float nai = -(t1[0] * ei + t1[1] * er);
      float b0r, b0i, b1r, b1i, b2r, b2i;
      cexp_p(t0[0], t0[1], t0[2], pB0, b0r, b0i);
      cexp_p(t0[0], t0[1], t0[2], pB1, b1r, b1i);
      cexp_p(t0[0], t0[1], t0[2], pB2, b2r, b2i);
      acc0 = __builtin_amdgcn_mfma_f32_16x16x4f32(ar, b0r, acc0, 0, 0, 0);
      acc1 = __builtin_amdgcn_mfma_f32_16x16x4f32(ar, b1r, acc1, 0, 0, 0);
      acc2 = __builtin_amdgcn_mfma_f32_16x16x4f32(ar, b2r, acc2, 0, 0, 0);
      acc0 = __builtin_amdgcn_mfma_f32_16x16x4f32(nai, b0i, acc0, 0, 0, 0);
      acc1 = __builtin_amdgcn_mfma_f32_16x16x4f32(nai, b1i, acc1, 0, 0, 0);
      acc2 = __builtin_amdgcn_mfma_f32_16x16x4f32(nai, b2i, acc2, 0, 0, 0);
    }
    *(f32x4*)(red + ((wv * 3 + 0) * 64 + lane) * 4) = acc0;
    *(f32x4*)(red + ((wv * 3 + 1) * 64 + lane) * 4) = acc1;
    *(f32x4*)(red + ((wv * 3 + 2) * 64 + lane) * 4) = acc2;
    __syncthreads();
    for (int e = tid; e < 768; e += 512) {
      float s = 0.f;
#pragma unroll
      for (int w = 0; w < 8; ++w) s += red[w * 768 + e];
      int c = e >> 8, ln = (e >> 2) & 63, reg = e & 3;
      Kd[c * 256 + (4 * (ln >> 4) + reg) * 16 + (ln & 15)] = s;
    }
    __syncthreads();
    {
      float v[4];
#pragma unroll
      for (int e = 0; e < 4; ++e) {
        int lag = 2047 - (4 * tid + e);
        int p = (int)bfr((float)lag);
        int c, row, col;
        if (p < 256) { c = 0; row = (p & 127) >> 3; col = 8 * (p >> 7) + (p & 7); }
        else if (p < 512) { int q = p - 256; c = 1; row = (q & 127) >> 3; col = 4 * (q >> 7) + ((q & 7) >> 1); }
        else if (p < 1024) { int q = p - 512; c = 1; row = (q & 127) >> 3; col = 8 + 2 * (q >> 7) + ((q & 7) >> 2); }
        else { int q = p - 1024; c = 2; row = (q & 127) >> 3; col = q >> 7; }
        v[e] = Kd[c * 256 + row * 16 + col];
      }
      u32x2 o = {pack2(v[0], v[1]), pack2(v[2], v[3])};
      *(u32x2*)(Kx + (size_t)d * LL + 4 * tid) = o;
    }
  } else if (bid < K1_KGEN_BLOCKS + K1_LN_BLOCKS) {
    const int l0 = (bid - K1_KGEN_BLOCKS) * 32;
    char* tu = smem;
    char* tx = smem + 28672;
    float uv[4][8], xv[4][8];
#pragma unroll
    for (int r = 0; r < 4; ++r) {
      const int l = l0 + wv * 4 + r;
      u32x4 raw = {0, 0, 0, 0};
      if (lane < 56) raw = *(const u32x4*)(x + (size_t)l * DD + lane * 8);
#pragma unroll
      for (int e = 0; e < 4; ++e) { xv[r][2 * e] = bflo(raw[e]); xv[r][2 * e + 1] = bfhi(raw[e]); }
    }
#pragma unroll
    for (int r = 0; r < 4; ++r) {
      float s = 0.f;
#pragma unroll
      for (int e = 0; e < 8; ++e) s += xv[r][e];
#pragma unroll
      for (int o = 32; o > 0; o >>= 1) s += __shfl_xor(s, o);
      float mean = s * (1.f / DD);
      float q = 0.f;
      if (lane < 56) {
#pragma unroll
        for (int e = 0; e < 8; ++e) { float t = xv[r][e] - mean; q += t * t; }
      }
#pragma unroll
      for (int o = 32; o > 0; o >>= 1) q += __shfl_xor(q, o);
      float m = bfr(mean);
      float v = bfr(q * (1.f / DD));
      float sd = bfr(sqrtf(bfr(v + 1e-5f)));
#pragma unroll
      for (int e = 0; e < 8; ++e) uv[r][e] = bfr(xv[r][e] - m) / sd;
    }
    if (lane < 56) {
#pragma unroll
      for (int e = 0; e < 8; ++e) {
        int dch = lane * 8 + e;
        int off = dch * 64 + ((wv ^ (lane & 7)) * 8);
        u32x2 a = {pack2(uv[0][e], uv[1][e]), pack2(uv[2][e], uv[3][e])};
        u32x2 b = {pack2(xv[0][e], xv[1][e]), pack2(xv[2][e], xv[3][e])};
        *(u32x2*)(tu + off) = a;
        *(u32x2*)(tx + off) = b;
      }
    }
    __syncthreads();
    for (int e = tid; e < DD * 4; e += 512) {
      int dch = e >> 2, c2 = (e & 3) * 2;
      int sw = (dch >> 3) & 7;
      u32x2 a0 = *(const u32x2*)(tu + dch * 64 + ((c2 ^ sw) * 8));
      u32x2 a1 = *(const u32x2*)(tu + dch * 64 + (((c2 + 1) ^ sw) * 8));
      u32x2 b0 = *(const u32x2*)(tx + dch * 64 + ((c2 ^ sw) * 8));
      u32x2 b1 = *(const u32x2*)(tx + dch * 64 + (((c2 + 1) ^ sw) * 8));
      u32x4 ao = {a0[0], a0[1], a1[0], a1[1]};
      u32x4 bo = {b0[0], b0[1], b1[0], b1[1]};
      *(u32x4*)(uT + (size_t)dch * LL + l0 + c2 * 4) = ao;
      *(u32x4*)(xT + (size_t)dch * LL + l0 + c2 * 4) = bo;
    }
  } else {
    const int f0 = (bid - K1_KGEN_BLOCKS - K1_LN_BLOCKS) * 128;
    float* red = (float*)smem;
    const int fc = tid & 15, rg = tid >> 4;
    float s[8] = {0, 0, 0, 0, 0, 0, 0, 0};
#pragma unroll
    for (int r = 0; r < 14; ++r) {
      u32x4 raw = *(const u32x4*)(w1 + (size_t)(rg * 14 + r) * FF + f0 + fc * 8);
#pragma unroll
      for (int e = 0; e < 4; ++e) { s[2 * e] += bflo(raw[e]); s[2 * e + 1] += bfhi(raw[e]); }
    }
#pragma unroll
    for (int e = 0; e < 8; ++e) red[rg * 128 + fc * 8 + e] = s[e];
    __syncthreads();
    if (tid < 128) {
      float t = 0.f;
#pragma unroll
      for (int r = 0; r < 32; ++r) t += red[r * 128 + tid];
      S1[f0 + tid] = t;
    }
  }
}

#define K2_CSTRIDE 4160
__global__ __launch_bounds__(256) void k2_kernel(
    const unsigned short* __restrict__ uT, const unsigned short* __restrict__ xT,
    const unsigned short* __restrict__ Kx, const unsigned short* __restrict__ Dskip,
    unsigned short* __restrict__ hT) {
  __shared__ __attribute__((aligned(16))) char smem[8 * K2_CSTRIDE + 4608];
  char* Kc = smem;
  char* Ul = smem + 8 * K2_CSTRIDE;
  const int tid = threadIdx.x, lane = tid & 63, wv = tid >> 6;
  const int d = blockIdx.x;
  {
    const u32x4* kp = (const u32x4*)(Kx + (size_t)d * LL);
    u32x4 w0 = kp[tid];
    u32x4 w1 = {0, 0, 0, 0};
    if (tid < 255) w1 = kp[tid + 1];
    u32x4 uu = *(const u32x4*)(uT + (size_t)d * LL + tid * 8);
    unsigned w[8] = {w0[0], w0[1], w0[2], w0[3], w1[0], w1[1], w1[2], w1[3]};
#pragma unroll
    for (int m = 0; m < 4; ++m) {
      u32x4 ev = {w[m], w[m + 1], w[m + 2], w[m + 3]};
      u32x4 od;
#pragma unroll
      for (int i = 0; i < 4; ++i) od[i] = __builtin_amdgcn_alignbit(w[m + i + 1], w[m + i], 16);
      *(u32x4*)(Kc + (2 * m) * K2_CSTRIDE + tid * 16) = ev;
      *(u32x4*)(Kc + (2 * m + 1) * K2_CSTRIDE + tid * 16) = od;
    }
    u32x4 z = {0, 0, 0, 0};
    if (tid < 8) *(u32x4*)(Kc + tid * K2_CSTRIDE + 4096) = z;
    if (tid < 32) *(u32x4*)(Ul + tid * 16) = z;
    *(u32x4*)(Ul + 512 + tid * 16) = uu;
  }
  __syncthreads();
  const int b = lane & 15, h = lane >> 4;
  f32x4 acc[8];
#pragma unroll
  for (int t = 0; t < 8; ++t) acc[t] = (f32x4){0, 0, 0, 0};
  const char* fbase = Kc + (7 - (b & 7)) * K2_CSTRIDE + 2 * (2040 - 8 * (b >> 3) + 8 * h);
  const char* gbase = Ul + 512 + 2 * (16 * b + 8 * h);
#pragma unroll
  for (int rr = 0; rr < 2; ++rr) {
    const int r = 4 * wv + 1 + 2 * rr;
    bf16x8 Fq[8], Gs[8];
#pragma unroll
    for (int q = 0; q < 8; ++q) Fq[q] = *(const bf16x8*)(fbase - 32 * (16 * q + r));
#pragma unroll
    for (int s = 0; s < 8; ++s) Gs[s] = *(const bf16x8*)(gbase + 32 * (16 * s - r));
#pragma unroll
    for (int q = 0; q < 8; ++q)
#pragma unroll
      for (int s = 0; s < 8 - q; ++s)
        acc[q + s] = __builtin_amdgcn_mfma_f32_16x16x32_bf16(Fq[q], Gs[s], acc[q + s], 0, 0, 0);
  }
  __syncthreads();
  float* red = (float*)smem;
#pragma unroll
  for (int t = 0; t < 8; ++t) *(f32x4*)(red + ((wv * 8 + t) * 64 + lane) * 4) = acc[t];
  __syncthreads();
  const float Dk = bf2f(Dskip[d]);
#pragma unroll
  for (int tt = 0; tt < 2; ++tt) {
    const int t = 2 * wv + tt;
    f32x4 y = *(const f32x4*)(red + ((0 * 8 + t) * 64 + lane) * 4);
#pragma unroll
    for (int w = 1; w < 4; ++w) y += *(const f32x4*)(red + ((w * 8 + t) * 64 + lane) * 4);
    const int l = 256 * t + 16 * b + 4 * h;
    u32x2 uu = *(const u32x2*)(Ul + 512 + 2 * l);
    u32x2 xx = *(const u32x2*)(xT + (size_t)d * LL + l);
    float u0 = bflo(uu[0]), u1 = bfhi(uu[0]), u2 = bflo(uu[1]), u3 = bfhi(uu[1]);
    float x0 = bflo(xx[0]), x1 = bfhi(xx[0]), x2 = bflo(xx[1]), x3 = bfhi(xx[1]);
    float h0 = x0 + bfr(bfr(y[0]) + bfr(u0 * Dk));
    float h1 = x1 + bfr(bfr(y[1]) + bfr(u1 * Dk));
    float h2 = x2 + bfr(bfr(y[2]) + bfr(u2 * Dk));
    float h3 = x3 + bfr(bfr(y[3]) + bfr(u3 * Dk));
    u32x2 o = {pack2(h0, h1), pack2(h2, h3)};
    *(u32x2*)(hT + (size_t)d * LL + l) = o;
  }
}

DI bf16x8 tr_frag(const char* p0, const char* p1) {
  s16x4 lo = __builtin_amdgcn_ds_read_tr16_b64_v4i16((lds_s16x4_ptr)p0);
  s16x4 hi = __builtin_amdgcn_ds_read_tr16_b64_v4i16((lds_s16x4_ptr)p1);
  typedef short s16x8 __attribute__((ext_vector_type(8)));
  s16x8 v = __builtin_shufflevector(lo, hi, 0, 1, 2, 3, 4, 5, 6, 7);
  return __builtin_bit_cast(bf16x8, v);
}

DI float gelu_tanh(float x) {
  const float c0 = -2.f * 0.7978845608028654f * 1.4426950408889634f;
  const float c1 = c0 * 0.044715f;
  float t = __builtin_fmaf(x * x, c1, c0);
  float e = __builtin_amdgcn_exp2f(x * t);
  return x * __builtin_amdgcn_rcpf(1.f + e);
}

#define K3_NBUF 4
#define K3_CHUNK 32768
__global__ __launch_bounds__(256) void k3_kernel(
    const unsigned short* __restrict__ hT, const unsigned short* __restrict__ w1,
    const float* __restrict__ S1, const unsigned short* __restrict__ b1,
    unsigned short* __restrict__ Hf) {
  extern __shared__ __attribute__((aligned(16))) char smem[];
  const int tid = threadIdx.x, lane = tid & 63, wv = tid >> 6;
  const int bid = blockIdx.x;
  const int xcd = bid & 7, idx = bid >> 3;
  const int lt = 2 * xcd + (idx / 14), ft = idx % 14;
  const int l0 = lt * 128, f0 = ft * 128;
  const int wf = wv >> 1, wl = wv & 1;

  const int srow = lane >> 4, sch = lane & 15;
  auto stage = [&](int c, int buf) {
    char* base = smem + buf * K3_CHUNK;
#pragma unroll
    for (int i = 0; i < 4; ++i) {
      const int row = (i * 4 + wv) * 4 + srow;
      const int ch = sch ^ ((row & 3) << 2);
      const int k = c * 64 + row;
      __builtin_amdgcn_global_load_lds((const unsigned*)(w1 + (size_t)k * FF + f0 + ch * 8),
                                       (unsigned*)(base + (i * 4 + wv) * 1024), 16, 0, 0);
      __builtin_amdgcn_global_load_lds((const unsigned*)(hT + (size_t)k * LL + l0 + ch * 8),
                                       (unsigned*)(base + 16384 + (i * 4 + wv) * 1024), 16, 0, 0);
    }
  };
  f32x16 acc[2][2];
#pragma unroll
  for (int a = 0; a < 2; ++a)
#pragma unroll
    for (int b = 0; b < 2; ++b)
#pragma unroll
      for (int e = 0; e < 16; ++e) acc[a][b][e] = 0.f;
  float sm[2] = {0.f, 0.f}, sq[2] = {0.f, 0.f};

  const int hh = lane >> 5, blk = (lane >> 4) & 1, q = (lane & 15) >> 2, p = lane & 3;
  int aoff[2], boff[2];
#pragma unroll
  for (int c = 0; c < 2; ++c) {
    aoff[c] = (8 * hh + q) * 256 + (4 * ((2 * wf + c) ^ q) + 2 * blk + (p >> 1)) * 16 + 8 * (p & 1);
    boff[c] = 16384 + (8 * hh + q) * 256 + (4 * ((2 * wl + c) ^ q) + 2 * blk + (p >> 1)) * 16 + 8 * (p & 1);
  }
  const bf16x2 ones = {(__bf16)1.0f, (__bf16)1.0f};

  stage(0, 0); stage(1, 1); stage(2, 2);
#pragma unroll
  for (int c = 0; c < 7; ++c) {
    if (c < 5) { WAIT_V_BAR(16); } else if (c == 5) { WAIT_V_BAR(8); } else { WAIT_V_BAR(0); }
    if (c + 3 < 7) stage(c + 3, (c + 3) & 3);
    const char* base = smem + (c & 3) * K3_CHUNK;
#pragma unroll
    for (int ks = 0; ks < 4; ++ks) {
      bf16x8 af[2], bfg[2];
#pragma unroll
      for (int t = 0; t < 2; ++t) {
        af[t] = tr_frag(base + aoff[t] + ks * 4096, base + aoff[t] + ks * 4096 + 1024);
        bfg[t] = tr_frag(base + boff[t] + ks * 4096, base + boff[t] + ks * 4096 + 1024);
      }
#pragma unroll
      for (int a = 0; a < 2; ++a)
#pragma unroll
        for (int b = 0; b < 2; ++b)
          acc[a][b] = __builtin_amdgcn_mfma_f32_32x32x16_bf16(af[a], bfg[b], acc[a][b], 0, 0, 0);
#pragma unroll
      for (int b = 0; b < 2; ++b)
#pragma unroll
        for (int e = 0; e < 4; ++e) {
          bf16x2 pr = {bfg[b][2 * e], bfg[b][2 * e + 1]};
          sm[b] = __builtin_amdgcn_fdot2_f32_bf16(pr, ones, sm[b], false);
          sq[b] = __builtin_amdgcn_fdot2_f32_bf16(pr, pr, sq[b], false);
        }
    }
  }
  float mB[2], rB[2];
#pragma unroll
  for (int b = 0; b < 2; ++b) {
    float s = sm[b] + __shfl_xor(sm[b], 32);
    float s2 = sq[b] + __shfl_xor(sq[b], 32);
    float mean = s * (1.f / DD);
    float var = fmaxf(s2 * (1.f / DD) - mean * mean, 0.f);
    mB[b] = bfr(mean);
    float sd = bfr(sqrtf(bfr(bfr(var) + 1e-5f)));
    rB[b] = 1.f / sd;
  }
#pragma unroll
  for (int a = 0; a < 2; ++a) {
    const int fb = f0 + wf * 64 + a * 32;
    float Sv[16], Bv[16];
#pragma unroll
    for (int g = 0; g < 4; ++g) {
      f32x4 s4 = *(const f32x4*)(S1 + fb + 8 * g + 4 * hh);
      u32x2 b2 = *(const u32x2*)(b1 + fb + 8 * g + 4 * hh);
      Sv[4 * g] = s4[0]; Sv[4 * g + 1] = s4[1]; Sv[4 * g + 2] = s4[2]; Sv[4 * g + 3] = s4[3];
      Bv[4 * g] = bflo(b2[0]); Bv[4 * g + 1] = bfhi(b2[0]); Bv[4 * g + 2] = bflo(b2[1]); Bv[4 * g + 3] = bfhi(b2[1]);
    }
#pragma unroll
    for (int b = 0; b < 2; ++b) {
      float g[16];
#pragma unroll
      for (int e = 0; e < 16; ++e) {
        float v = __builtin_fmaf(__builtin_fmaf(-mB[b], Sv[e], acc[a][b][e]), rB[b], Bv[e]);
        g[e] = gelu_tanh(v);
      }
      const int ltile = (l0 + wl * 64 + b * 32) >> 5;
      const int fstep = fb >> 4;
#pragma unroll
      for (int s = 0; s < 2; ++s) {
        u32x4 o = {pack2(g[8 * s], g[8 * s + 1]), pack2(g[8 * s + 2], g[8 * s + 3]),
                   pack2(g[8 * s + 4], g[8 * s + 5]), pack2(g[8 * s + 6], g[8 * s + 7])};
        *(u32x4*)(Hf + (((size_t)ltile * (FF / 16) + fstep + s) * 64 + lane) * 8) = o;
      }
    }
  }
}

#define K4_NBUF 4
#define K4_CHUNK 32768
__global__ __launch_bounds__(256) void k4_kernel(
    const unsigned short* __restrict__ Hf, const unsigned short* __restrict__ w2,
    const unsigned short* __restrict__ b2, const unsigned short* __restrict__ hT,
    unsigned short* __restrict__ out) {
  extern __shared__ __attribute__((aligned(16))) char smem[];
  const int tid = threadIdx.x, lane = tid & 63, wv = tid >> 6;
  const int bid = blockIdx.x;
  const int xcd = bid & 7, idx = bid >> 3;
  const int lt = 4 * xcd + (idx / 7), dt = idx % 7;
  const int l0 = lt * 64, d0 = dt * 64;
  const int wl = wv >> 1, wd = wv & 1;

  const int srow = lane >> 3, sch = lane & 7;
  auto stage = [&](int c, int buf) {
    char* base = smem + buf * K4_CHUNK;
#pragma unroll
    for (int i = 0; i < 4; ++i) {
      const int piece = i * 4 + wv;
      const int ltile = (l0 >> 5) + (piece >> 3);
      __builtin_amdgcn_global_load_lds(
          (const unsigned*)(Hf + (((size_t)ltile * (FF / 16) + c * 8 + (piece & 7)) * 64 + lane) * 8),
          (unsigned*)(base + piece * 1024), 16, 0, 0);
      const int row = piece * 8 + srow;
      const int ch = sch ^ (((row >> 1) & 1) << 2);
      __builtin_amdgcn_global_load_lds((const unsigned*)(w2 + (size_t)(c * 128 + row) * DD + d0 + ch * 8),
                                       (unsigned*)(base + 16384 + piece * 1024), 16, 0, 0);
    }
  };
  f32x16 acc;
#pragma unroll
  for (int e = 0; e < 16; ++e) acc[e] = 0.f;
  const int hh = lane >> 5, blk = (lane >> 4) & 1, q = (lane & 15) >> 2, p = lane & 3;
  const int boff = 16384 + (4 * hh + q) * 128 + ((4 * wd + 2 * blk + (p >> 1)) ^ (((q >> 1) & 1) << 2)) * 16 + 8 * (p & 1);
  const int aoff = wl * 8192 + lane * 16;

  stage(0, 0); stage(1, 1); stage(2, 2);
  for (int c = 0; c < 14; ++c) {
    if (c < 12) { WAIT_V_BAR(16); } else if (c == 12) { WAIT_V_BAR(8); } else { WAIT_V_BAR(0); }
    if (c + 3 < 14) stage(c + 3, (c + 3) & 3);
    const char* base = smem + (c & 3) * K4_CHUNK;
#pragma unroll
    for (int ks = 0; ks < 8; ++ks) {
      bf16x8 af = *(const bf16x8*)(base + aoff + ks * 1024);
      bf16x8 bfg = tr_frag(base + boff + ks * 2048, base + boff + ks * 2048 + 1024);
      acc = __builtin_amdgcn_mfma_f32_32x32x16_bf16(af, bfg, acc, 0, 0, 0);
    }
  }
  const int dcol = d0 + wd * 32 + (lane & 31);
  const float bias = bf2f(b2[dcol]);
#pragma unroll
  for (int g = 0; g < 4; ++g) {
    const int l = l0 + wl * 32 + 8 * g + 4 * hh;
    u32x2 hv = *(const u32x2*)(hT + (size_t)dcol * LL + l);
    float hx[4] = {bflo(hv[0]), bfhi(hv[0]), bflo(hv[1]), bfhi(hv[1])};
#pragma unroll
    for (int e = 0; e < 4; ++e) {
      float o = hx[e] + (acc[4 * g + e] + bias);
      __bf16 ob = (__bf16)o;
      out[(size_t)(l + e) * DD + dcol] = __builtin_bit_cast(unsigned short, ob);
    }
  }
}

extern "C" void kernel_launch(void* const* d_in, const int* in_sizes, int n_in, void* d_out,
                              int out_size, void* d_ws, size_t ws_size, hipStream_t stream) {
  const unsigned short* x = (const unsigned short*)d_in[0];
  const unsigned short* logA = (const unsigned short*)d_in[1];
  const unsigned short* Aim = (const unsigned short*)d_in[2];
  const unsigned short* Cre = (const unsigned short*)d_in[3];
  const unsigned short* Cim = (const unsigned short*)d_in[4];
  const unsigned short* logdt = (const unsigned short*)d_in[5];
  const unsigned short* Dskip = (const unsigned short*)d_in[6];
  const unsigned short* w1 = (const unsigned short*)d_in[8];
  const unsigned short* b1 = (const unsigned short*)d_in[9];
  const unsigned short* w2 = (const unsigned short*)d_in[10];
  const unsigned short* b2 = (const unsigned short*)d_in[11];
  unsigned short* out = (unsigned short*)d_out;

  char* ws = (char*)d_ws;
  const size_t T = (size_t)DD * LL * 2;
  unsigned short* uT = (unsigned short*)(ws);
  unsigned short* xT = (unsigned short*)(ws + T);
  unsigned short* Kx = (unsigned short*)(ws + 2 * T);
  unsigned short* hT = (unsigned short*)(ws + 3 * T);
  float* S1 = (float*)(ws + 4 * T);
  unsigned short* Hf = (unsigned short*)(ws + 4 * T + 8192);

  hipFuncSetAttribute((const void*)k3_kernel, hipFuncAttributeMaxDynamicSharedMemorySize, K3_NBUF * K3_CHUNK);
  hipFuncSetAttribute((const void*)k4_kernel, hipFuncAttributeMaxDynamicSharedMemorySize, K4_NBUF * K4_CHUNK);

  k1_kernel<<<K1_KGEN_BLOCKS + K1_LN_BLOCKS + K1_CS_BLOCKS, 512, 0, stream>>>(x, logA, Aim, Cre, Cim, logdt, w1, uT, xT, Kx, S1);
  k2_kernel<<<DD, 256, 0, stream>>>(uT, xT, Kx, Dskip, hT);
  k3_kernel<<<224, 256, K3_NBUF * K3_CHUNK, stream>>>(hT, w1, S1, b1, Hf);
  k4_kernel<<<224, 256, K4_NBUF * K4_CHUNK, stream>>>(Hf, w2, b2, hT, out);
}
```

```cpp
#include <hip/hip_runtime.h>
#include <hip/hip_bf16.h>
#include <stdint.h>

#define LL 2048
#define DD 448
#define NN 256
#define FF 1792

typedef __bf16 bf16x2 __attribute__((ext_vector_type(2)));
typedef __bf16 bf16x8 __attribute__((ext_vector_type(8)));
typedef _Float16 f16x2 __attribute__((ext_vector_type(2)));
typedef _Float16 f16x8 __attribute__((ext_vector_type(8)));
typedef float f32x2 __attribute__((ext_vector_type(2)));
typedef float f32x4 __attribute__((ext_vector_type(4)));
typedef float f32x16 __attribute__((ext_vector_type(16)));
typedef unsigned u32x4 __attribute__((ext_vector_type(4)));
typedef unsigned u32x2 __attribute__((ext_vector_type(2)));

#define DI __device__ __forceinline__

#define STAMP(k, i)

DI float bfr(float x) { return (float)(__bf16)x; }
DI float bf2f(unsigned short v) { return __uint_as_float(((unsigned)v) << 16); }
DI float bflo(unsigned w) { return __uint_as_float(w << 16); }
DI float bfhi(unsigned w) { return __uint_as_float(w & 0xffff0000u); }
DI unsigned pack2(float a, float b) {
  bf16x2 r = {(__bf16)a, (__bf16)b};
  return __builtin_bit_cast(unsigned, r);
}
DI unsigned packh(f32x2 a) {
  f16x2 r = {(_Float16)a.x, (_Float16)a.y};
  return __builtin_bit_cast(unsigned, r);
}
DI unsigned lds_addr(const void* p) {
  return (unsigned)(uintptr_t)(__attribute__((address_space(3))) const char*)p;
}

#define WAIT_V_BAR(n) asm volatile("s_waitcnt vmcnt(" #n ") lgkmcnt(0)\n\ts_barrier" ::: "memory")
#define WAIT_L0() asm volatile("s_waitcnt lgkmcnt(0)" ::: "memory")
#define DS_TR(dst, addr, off) asm volatile("ds_read_b64_tr_b16 %0, %1 offset:%2" : "=v"(dst) : "v"(addr), "i"(off))
#define DS_B128(dst, addr, off) asm volatile("ds_read_b128 %0, %1 offset:%2" : "=v"(dst) : "v"(addr), "i"(off))

DI bf16x8 cat_frag(u32x2 lo, u32x2 hi) {
  u32x4 v = __builtin_shufflevector(lo, hi, 0, 1, 2, 3);
  return __builtin_bit_cast(bf16x8, v);
}

#define K1_LN_BLOCKS 64
#define K1_KG_BLOCKS DD
#define K1_CS_BLOCKS 28
#define K1_PF_BLOCKS 16
#define K1_ROWB 272
#define K1_WREG (48 * K1_ROWB)
#define K1_LNROW 928

DI f32x2 cexp_p(float lr2, float lih, float lil, float p) {
  float mag = __builtin_amdgcn_exp2f(lr2 * p);
  float ph = lih * p;
  float e = __builtin_fmaf(lih, p, -ph);
  float fr = __builtin_amdgcn_fractf(ph) + __builtin_fmaf(lil, p, e);
  f32x2 cs = {__builtin_amdgcn_cosf(fr), __builtin_amdgcn_sinf(fr)};
  return cs * mag;
}
DI f32x2 crot(f32x2 w) { return (f32x2){-w.y, w.x}; }
DI f32x2 cmul(f32x2 a, f32x2 w, f32x2 wr) { return __builtin_elementwise_fma(a.yy, wr, a.xx * w); }
DI int cidx_of_lag(int lag) {
  __bf16 b = (__bf16)(float)lag;
  int bits = (int)__builtin_bit_cast(unsigned short, b);
  return lag < 128 ? lag : bits - (0x4300 - 128);
}
DI float sum8(float v) {
  v += __builtin_bit_cast(float, __builtin_amdgcn_mov_dpp(__builtin_bit_cast(int, v), 0xB1, 0xF, 0xF, true));
  v += __builtin_bit_cast(float, __builtin_amdgcn_mov_dpp(__builtin_bit_cast(int, v), 0x4E, 0xF, 0xF, true));
  v += __builtin_bit_cast(float, __builtin_amdgcn_mov_dpp(__builtin_bit_cast(int, v), 0x141, 0xF, 0xF, true));
  return v;
}
typedef short s16x4 __attribute__((ext_vector_type(4)));
typedef __attribute__((address_space(3))) s16x4* lds_s16x4_ptr;

__global__ __launch_bounds__(256) void k1_kernel(
    const unsigned short* __restrict__ x, const unsigned short* __restrict__ logA,
    const unsigned short* __restrict__ Aim, const unsigned short* __restrict__ Cre,
    const unsigned short* __restrict__ Cim, const unsigned short* __restrict__ logdt,
    const unsigned short* __restrict__ w1, const unsigned short* __restrict__ w2,
    unsigned short* __restrict__ uT, unsigned short* __restrict__ xT,
    unsigned short* __restrict__ Kx, float* __restrict__ S1) {
  __shared__ __attribute__((aligned(16))) char smem[4 * K1_WREG];
  const int tid = threadIdx.x, lane = tid & 63, wv = __builtin_amdgcn_readfirstlane(tid >> 6);
  const int bid = blockIdx.x;
  STAMP(0, 0);
  if (bid < K1_LN_BLOCKS) {
    const int l0 = bid * 32;
    const int r = lane >> 3, c = lane & 7;
    const int row = wv * 8 + r;
    u32x4 raw[7];
#pragma unroll
    for (int i = 0; i < 7; ++i) raw[i] = *(const u32x4*)(x + (size_t)(l0 + row) * DD + (c + 8 * i) * 8);
    f32x2 s2 = {0.f, 0.f};
#pragma unroll
    for (int i = 0; i < 7; ++i)
#pragma unroll
      for (int e = 0; e < 4; ++e) s2 += (f32x2){bflo(raw[i][e]), bfhi(raw[i][e])};
    const float mean = sum8(s2.x + s2.y) * (1.f / DD);
    f32x2 q2 = {0.f, 0.f};
    const f32x2 mm = {mean, mean};
#pragma unroll
    for (int i = 0; i < 7; ++i)
#pragma unroll
      for (int e = 0; e < 4; ++e) { f32x2 t = (f32x2){bflo(raw[i][e]), bfhi(raw[i][e])} - mm; q2 = __builtin_elementwise_fma(t, t, q2); }
    const float var = sum8(q2.x + q2.y) * (1.f / DD);
    const float m = bfr(mean);
    const float sd = bfr(sqrtf(bfr(bfr(var) + 1e-5f)));
    const float rs = 1.f / sd;
    const f32x2 m2 = {m, m}, rs2 = {rs, rs}, nsd2 = {-sd, -sd};
    char* tu = smem;
#pragma unroll
    for (int i = 0; i < 7; ++i) {
      u32x4 o;
#pragma unroll
      for (int e = 0; e < 4; ++e) {
        f32x2 t = (f32x2){bflo(raw[i][e]), bfhi(raw[i][e])} - m2;
        unsigned tb = pack2(t.x, t.y);
        f32x2 dd = {bflo(tb), bfhi(tb)};
        f32x2 qv = dd * rs2;
        f32x2 er = __builtin_elementwise_fma(qv, nsd2, dd);
        qv = __builtin_elementwise_fma(er, rs2, qv);
        o[e] = pack2(qv.x, qv.y);
      }
      *(u32x4*)(tu + row * K1_LNROW + (c + 8 * i) * 16) = o;
    }
    __syncthreads();
    const int g = lane >> 4, q = (lane & 15) >> 2, pp = lane & 3;
    const char* rb = tu + (8 * g + q) * K1_LNROW + pp * 8;
#pragma unroll
    for (int it = 0; it < 7; ++it) {
      const int c0 = (it * 4 + wv) * 16;
      s16x4 a = __builtin_amdgcn_ds_read_tr16_b64_v4i16((lds_s16x4_ptr)(rb + c0 * 2));
      s16x4 b = __builtin_amdgcn_ds_read_tr16_b64_v4i16((lds_s16x4_ptr)(rb + 4 * K1_LNROW + c0 * 2));
      u32x2 a2 = __builtin_bit_cast(u32x2, a), b2 = __builtin_bit_cast(u32x2, b);
      u32x4 o = {a2[0], a2[1], b2[0], b2[1]};
      *(u32x4*)(uT + (size_t)(c0 + (lane & 15)) * LL + l0 + 8 * g) = o;
    }
    __syncthreads();
#pragma unroll
    for (int i = 0; i < 7; ++i) *(u32x4*)(tu + row * K1_LNROW + (c + 8 * i) * 16) = raw[i];
    __syncthreads();
#pragma unroll
    for (int it = 0; it < 7; ++it) {
      const int c0 = (it * 4 + wv) * 16;
      s16x4 a = __builtin_amdgcn_ds_read_tr16_b64_v4i16((lds_s16x4_ptr)(rb + c0 * 2));
      s16x4 b = __builtin_amdgcn_ds_read_tr16_b64_v4i16((lds_s16x4_ptr)(rb + 4 * K1_LNROW + c0 * 2));
      u32x2 a2 = __builtin_bit_cast(u32x2, a), b2 = __builtin_bit_cast(u32x2, b);
      u32x4 o = {a2[0], a2[1], b2[0], b2[1]};
      *(u32x4*)(xT + (size_t)(c0 + (lane & 15)) * LL + l0 + 8 * g) = o;
    }
  } else if (bid < K1_LN_BLOCKS + K1_KG_BLOCKS) {
    const int d = bid - K1_LN_BLOCKS;
    const int nb = d * NN + tid;
    float lr2, lih, lil, cr, ci;
    {
      const unsigned short la = logA[nb], ai = Aim[nb], cre = Cre[nb], cim = Cim[nb];
      const float dt = bfr(expf(bf2f(logdt[d])));
      const float I2PH = 0.15915494f;
      const float I2PL = 6.4206382e-09f;
      const float lr = -bfr(expf(bf2f(la))) * dt;
      const float li = bf2f(ai) * dt;
      lr2 = lr * 1.44269504f;
      lih = li * I2PH;
      lil = __builtin_fmaf(li, I2PH, -lih) + li * I2PL;
      cr = 2.f * bf2f(cre) * dt;
      ci = 2.f * bf2f(cim) * dt;
    }
    float cm = fmaxf(fabsf(cr), fabsf(ci));
#pragma unroll
    for (int o = 32; o > 0; o >>= 1) cm = fmaxf(cm, __shfl_xor(cm, o));
    const float scale = cm > 0.f ? cm : 1.f;
    const float inv = 1.f / scale;
    STAMP(0, 1);
    char* reg = smem + wv * K1_WREG;
    unsigned* wr_ = (unsigned*)(reg + lane * 4);
#define KROW(r) wr_[(r) * (K1_ROWB / 4)]
    const int i16 = lane & 15, kg = lane >> 4;
    const char* rdA = reg + i16 * K1_ROWB + kg * 16;
    f32x4 acc0 = {0, 0, 0, 0}, acc1 = {0, 0, 0, 0}, acc2 = {0, 0, 0, 0};
    {
      const f32x2 E1 = cexp_p(lr2, lih, lil, 1.f), E8 = cexp_p(lr2, lih, lil, 8.f), E128 = cexp_p(lr2, lih, lil, 128.f);
      const f32x2 E256 = cexp_p(lr2, lih, lil, 256.f), E512 = cexp_p(lr2, lih, lil, 512.f), E1024 = cexp_p(lr2, lih, lil, 1024.f);
      const f32x2 E1r = crot(E1), E128r = crot(E128), E256r = crot(E256), E512r = crot(E512);
      f32x2 a[16];
      {
        const f32x2 w = {E8.x, -E8.y}, wr = crot(w);
        const f32x2 w2 = cmul(w, w, wr), w2r = crot(w2);
        const f32x2 w4 = cmul(w2, w2, w2r), w4r = crot(w4);
        a[0] = (f32x2){cr * inv, -ci * inv};
        a[1] = cmul(a[0], w, wr);
        a[2] = cmul(a[0], w2, w2r);
        a[3] = cmul(a[1], w2, w2r);
#pragma unroll
        for (int i = 4; i < 16; ++i) a[i] = cmul(a[i - 4], w4, w4r);
      }
      f32x2 e[8];
      e[0] = (f32x2){1.f, 0.f}; e[1] = E1;
      e[2] = cmul(E1, E1, E1r);
      e[3] = cmul(e[2], E1, E1r);
      const f32x2 e2r = crot(e[2]);
      e[4] = cmul(e[2], e[2], e2r);
      const f32x2 e4r = crot(e[4]);
      e[5] = cmul(e[1], e[4], e4r); e[6] = cmul(e[2], e[4], e4r); e[7] = cmul(e[3], e[4], e4r);
#pragma unroll
      for (int i = 0; i < 16; ++i) KROW(i) = packh(a[i]);
#pragma unroll
      for (int j = 0; j < 8; ++j) { KROW(16 + j) = packh(e[j]); KROW(24 + j) = packh(j ? cmul(e[j], E128, E128r) : E128); }
      const f32x2 E384 = cmul(E256, E128, E128r), E384r = crot(E384);
#pragma unroll
      for (int r = 0; r < 4; ++r) {
        KROW(32 + r) = packh(r ? cmul(e[2 * r], E256, E256r) : E256);
        KROW(36 + r) = packh(r ? cmul(e[2 * r], E384, E384r) : E384);
      }
      f32x2 u[4];
      u[0] = E512; u[1] = cmul(E512, E128, E128r); u[2] = cmul(E512, E256, E256r); u[3] = cmul(E512, E384, E384r);
#pragma unroll
      for (int w = 0; w < 4; ++w) { KROW(40 + 2 * w) = packh(u[w]); KROW(41 + 2 * w) = packh(cmul(u[w], e[4], e4r)); }
      f32x2 v[9];
      v[0] = E1024; v[1] = cmul(E1024, E128, E128r); v[2] = cmul(E1024, E256, E256r); v[3] = cmul(E1024, E384, E384r);
#pragma unroll
      for (int c = 4; c < 9; ++c) v[c] = cmul(v[c - 4], E512, E512r);
      unsigned vp[9];
#pragma unroll
      for (int c = 0; c < 9; ++c) vp[c] = packh(v[c]);
      __builtin_amdgcn_fence(__ATOMIC_RELEASE, "wavefront");
      __builtin_amdgcn_wave_barrier();
      __builtin_amdgcn_fence(__ATOMIC_ACQUIRE, "wavefront");
      f16x8 af[4];
#pragma unroll
      for (int s = 0; s < 4; ++s) {
        af[s] = *(const f16x8*)(rdA + 64 * s);
        f16x8 b0 = *(const f16x8*)(rdA + 16 * K1_ROWB + 64 * s);
        f16x8 b1 = *(const f16x8*)(rdA + 32 * K1_ROWB + 64 * s);
        acc0 = __builtin_amdgcn_mfma_f32_16x16x32_f16(af[s], b0, acc0, 0, 0, 0);
        acc1 = __builtin_amdgcn_mfma_f32_16x16x32_f16(af[s], b1, acc1, 0, 0, 0);
      }
      __builtin_amdgcn_fence(__ATOMIC_RELEASE, "wavefront");
      __builtin_amdgcn_wave_barrier();
      __builtin_amdgcn_fence(__ATOMIC_ACQUIRE, "wavefront");
#pragma unroll
      for (int c = 0; c < 9; ++c) KROW(16 + c) = vp[c];
      __builtin_amdgcn_fence(__ATOMIC_RELEASE, "wavefront");
      __builtin_amdgcn_wave_barrier();
      __builtin_amdgcn_fence(__ATOMIC_ACQUIRE, "wavefront");
#pragma unroll
      for (int s = 0; s < 4; ++s) {
        f16x8 b2 = *(const f16x8*)(rdA + 16 * K1_ROWB + 64 * s);
        acc2 = __builtin_amdgcn_mfma_f32_16x16x32_f16(af[s], b2, acc2, 0, 0, 0);
      }
    }
#undef KROW
    acc0 *= scale; acc1 *= scale; acc2 *= scale;
    __syncthreads();
    STAMP(0, 2);
    float* red = (float*)smem;
    unsigned short* kd = (unsigned short*)(smem + 12288);
    *(f32x4*)(red + ((wv * 3 + 0) * 64 + lane) * 4) = acc0;
    *(f32x4*)(red + ((wv * 3 + 1) * 64 + lane) * 4) = acc1;
    *(f32x4*)(red + ((wv * 3 + 2) * 64 + lane) * 4) = acc2;
    __syncthreads();
    if (wv < 3) {
      f32x4 t = *(const f32x4*)(red + ((0 * 3 + wv) * 64 + lane) * 4);
#pragma unroll
      for (int w = 1; w < 4; ++w) t += *(const f32x4*)(red + ((w * 3 + wv) * 64 + lane) * 4);
      const int j = i16;
      const int lo0 = 128 * (j >> 3) + (j & 7);
      const int lo1 = j < 8 ? 256 + 128 * (j >> 2) + 2 * (j & 3) : 512 + 128 * ((j - 8) >> 1) + 4 * ((j - 8) & 1);
      const int lo2 = 1024 + 128 * j;
      const int lo = wv == 0 ? lo0 : (wv == 1 ? lo1 : lo2);
#pragma unroll
      for (int r = 0; r < 4; ++r) {
        const int pos = 8 * (4 * kg + r) + lo;
        __bf16 v0 = (__bf16)t[r];
        if (pos <= 2048) kd[cidx_of_lag(pos)] = __builtin_bit_cast(unsigned short, v0);
      }
    }
    __syncthreads();
    {
      unsigned o[4];
#pragma unroll
      for (int e = 0; e < 4; ++e) {
        const int lag = 2047 - (8 * tid + 2 * e);
        unsigned a = kd[cidx_of_lag(lag)], b = kd[cidx_of_lag(lag - 1)];
        o[e] = a | (b << 16);
      }
      u32x4 o0 = {o[0], o[1], o[2], o[3]};
      *(u32x4*)(Kx + (size_t)d * LL + 8 * tid) = o0;
    }
  } else if (bid < K1_LN_BLOCKS + K1_KG_BLOCKS + K1_CS_BLOCKS) {
    const int f0 = (bid - K1_KG_BLOCKS - K1_LN_BLOCKS) * 64;
    float* red = (float*)smem;
    const int fc = tid & 7, rg = tid >> 3;
    u32x4 raw[14];
#pragma unroll
    for (int r = 0; r < 14; ++r) raw[r] = *(const u32x4*)(w1 + (size_t)(rg * 14 + r) * FF + f0 + fc * 8);
    f32x2 s[4] = {{0.f, 0.f}, {0.f, 0.f}, {0.f, 0.f}, {0.f, 0.f}};
#pragma unroll
    for (int r = 0; r < 14; ++r)
#pragma unroll
      for (int e = 0; e < 4; ++e) s[e] += (f32x2){bflo(raw[r][e]), bfhi(raw[r][e])};
    *(f32x4*)(red + rg * 64 + fc * 8) = (f32x4){s[0].x, s[0].y, s[1].x, s[1].y};
    *(f32x4*)(red + rg * 64 + fc * 8 + 4) = (f32x4){s[2].x, s[2].y, s[3].x, s[3].y};
    __syncthreads();
    if (tid < 64) {
      float t = 0.f;
#pragma unroll
      for (int r = 0; r < 32; ++r) t += red[r * 64 + tid];
      S1[f0 + tid] = t;
    }
  } else {
    const int pb = bid - (K1_LN_BLOCKS + K1_KG_BLOCKS + K1_CS_BLOCKS);
    const u32x4* src = (const u32x4*)w2 + (size_t)pb * (FF * DD / 8 / K1_PF_BLOCKS) + tid;
    u32x4 acc = {0, 0, 0, 0};
#pragma unroll
    for (int i = 0; i < 24; ++i) acc ^= src[i * 256];
    if (tid < 128) acc ^= src[24 * 256];
    asm volatile("" ::"v"(acc));
  }
  STAMP(0, 3);
}

#define K2_CSTRIDE 4160
__global__ __launch_bounds__(256) void k2_kernel(
    const unsigned short* __restrict__ uT, const unsigned short* __restrict__ xT,
    const unsigned short* __restrict__ Kx, const unsigned short* __restrict__ Dskip,
    unsigned short* __restrict__ hT) {
  __shared__ __attribute__((aligned(16))) char smem[8 * K2_CSTRIDE + 4608];
  char* Kc = smem;
  char* Ul = smem + 8 * K2_CSTRIDE;
  const int tid = threadIdx.x, lane = tid & 63, wv = __builtin_amdgcn_readfirstlane(tid >> 6);
  const int d = blockIdx.x;
  const int b = lane & 15, h = lane >> 4;
  STAMP(1, 0);
  u32x2 xx[2];
  float Dk;
  {
    const u32x4* kp = (const u32x4*)(Kx + (size_t)d * LL);
    u32x4 w0 = kp[tid];
    u32x4 w1 = {0, 0, 0, 0};
    if (tid < 255) w1 = kp[tid + 1];
    u32x4 uu = *(const u32x4*)(uT + (size_t)d * LL + tid * 8);
#pragma unroll
    for (int tt = 0; tt < 2; ++tt) xx[tt] = *(const u32x2*)(xT + (size_t)d * LL + 256 * (2 * wv + tt) + 16 * b + 4 * h);
    Dk = bf2f(Dskip[d]);
    unsigned w[8] = {w0[0], w0[1], w0[2], w0[3], w1[0], w1[1], w1[2], w1[3]};
#pragma unroll
    for (int m = 0; m < 4; ++m) {
      u32x4 ev = {w[m], w[m + 1], w[m + 2], w[m + 3]};
      u32x4 od;
#pragma unroll
      for (int i = 0; i < 4; ++i) od[i] = __builtin_amdgcn_alignbit(w[m + i + 1], w[m + i], 16);
      *(u32x4*)(Kc + (2 * m) * K2_CSTRIDE + tid * 16) = ev;
      *(u32x4*)(Kc + (2 * m + 1) * K2_CSTRIDE + tid * 16) = od;
    }
    u32x4 z = {0, 0, 0, 0};
    if (tid < 8) *(u32x4*)(Kc + tid * K2_CSTRIDE + 4096) = z;
    if (tid < 32) *(u32x4*)(Ul + tid * 16) = z;
    *(u32x4*)(Ul + 512 + tid * 16) = uu;
  }
  __syncthreads();
  STAMP(1, 1);
  f32x4 acc[8];
#pragma unroll
  for (int t = 0; t < 8; ++t) acc[t] = (f32x4){0, 0, 0, 0};
  const char* fbase = Kc + (7 - (b & 7)) * K2_CSTRIDE + 2 * (2040 - 8 * (b >> 3) + 8 * h);
  const char* gbase = Ul + 512 + 2 * (16 * b + 8 * h);
#pragma unroll
  for (int rr = 0; rr < 2; ++rr) {
    const int r = 4 * wv + 1 + 2 * rr;
    bf16x8 Fq[8], Gs[8];
#pragma unroll
    for (int q = 0; q < 8; ++q) Fq[q] = *(const bf16x8*)(fbase - 32 * (16 * q + r));
#pragma unroll
    for (int s = 0; s < 8; ++s) Gs[s] = *(const bf16x8*)(gbase + 32 * (16 * s - r));
#pragma unroll
    for (int q = 0; q < 8; ++q)
#pragma unroll
      for (int s = 0; s < 8 - q; ++s)
        acc[q + s] = __builtin_amdgcn_mfma_f32_16x16x32_bf16(Fq[q], Gs[s], acc[q + s], 0, 0, 0);
  }
  __syncthreads();
  STAMP(1, 2);
  float* red = (float*)smem;
#pragma unroll
  for (int t = 0; t < 8; ++t) *(f32x4*)(red + ((wv * 8 + t) * 64 + lane) * 4) = acc[t];
  __syncthreads();
#pragma unroll
  for (int tt = 0; tt < 2; ++tt) {
    const int t = 2 * wv + tt;
    f32x4 y = *(const f32x4*)(red + ((0 * 8 + t) * 64 + lane) * 4);
#pragma unroll
    for (int w = 1; w < 4; ++w) y += *(const f32x4*)(red + ((w * 8 + t) * 64 + lane) * 4);
    const int l = 256 * t + 16 * b + 4 * h;
    u32x2 uu = *(const u32x2*)(Ul + 512 + 2 * l);
    float u0 = bflo(uu[0]), u1 = bfhi(uu[0]), u2 = bflo(uu[1]), u3 = bfhi(uu[1]);
    float x0 = bflo(xx[tt][0]), x1 = bfhi(xx[tt][0]), x2 = bflo(xx[tt][1]), x3 = bfhi(xx[tt][1]);
    float h0 = x0 + bfr(bfr(y[0]) + bfr(u0 * Dk));
    float h1 = x1 + bfr(bfr(y[1]) + bfr(u1 * Dk));
    float h2 = x2 + bfr(bfr(y[2]) + bfr(u2 * Dk));
    float h3 = x3 + bfr(bfr(y[3]) + bfr(u3 * Dk));
    u32x2 o = {pack2(h0, h1), pack2(h2, h3)};
    *(u32x2*)(hT + (size_t)d * LL + l) = o;
  }
  STAMP(1, 3);
}

#define K3_NBUF 4
#define K3_CHUNK 32768
#define K3_XOFF (K3_NBUF * K3_CHUNK)
#define K3_LDS (K3_XOFF + 4096)
__global__ __launch_bounds__(512) void k3_kernel(
    const unsigned short* __restrict__ hT, const unsigned short* __restrict__ w1,
    const float* __restrict__ S1, const unsigned short* __restrict__ b1,
    unsigned short* __restrict__ Hf) {
  extern __shared__ __attribute__((aligned(16))) char smem[];
  const int tid = threadIdx.x, lane = tid & 63, wv = __builtin_amdgcn_readfirstlane(tid >> 6);
  const int bid = blockIdx.x;
  const int xcd = bid & 7, idx = bid >> 3;
  const int lt = 2 * xcd + (idx / 14), ft = idx % 14;
  const int l0 = lt * 128, f0 = ft * 128;
  const int kg = wv >> 2;
  const int wf = (wv >> 1) & 1, wl = wv & 1;

  const int srow = lane >> 4, sch = lane & 15;
  const unsigned short* wsrc = w1 + (size_t)(wv * 4 + srow) * FF + f0 + (sch ^ ((srow & 3) << 2)) * 8;
  const unsigned short* hsrc = hT + (size_t)(wv * 4 + srow) * LL + l0 + (sch ^ ((srow & 3) << 2)) * 8;
  auto stage = [&](int c, int buf) {
    char* base = smem + buf * K3_CHUNK;
#pragma unroll
    for (int i = 0; i < 2; ++i) {
      const int k = c * 64 + i * 32;
      __builtin_amdgcn_global_load_lds((const unsigned*)(wsrc + (size_t)k * FF),
                                       (unsigned*)(base + (i * 8 + wv) * 1024), 16, 0, 0);
      __builtin_amdgcn_global_load_lds((const unsigned*)(hsrc + (size_t)k * LL),
                                       (unsigned*)(base + 16384 + (i * 8 + wv) * 1024), 16, 0, 0);
    }
  };
  STAMP(2, 0);
  if (wv < 2) __builtin_amdgcn_global_load_lds((const unsigned*)(S1 + f0 + tid), (unsigned*)(smem + K3_XOFF + wv * 256), 4, 0, 0);
  else if (wv == 2) __builtin_amdgcn_global_load_lds((const unsigned*)(b1 + f0 + lane * 2), (unsigned*)(smem + K3_XOFF + 512), 4, 0, 0);
  stage(0, 0); stage(1, 1); stage(2, 2); stage(3, 3);

  f32x16 acc[2][2];
#pragma unroll
  for (int a = 0; a < 2; ++a)
#pragma unroll
    for (int b = 0; b < 2; ++b)
#pragma unroll
      for (int e = 0; e < 16; ++e) acc[a][b][e] = 0.f;
  float sm = 0.f, sq = 0.f;

  const int hh = lane >> 5, blk = (lane >> 4) & 1, q = (lane & 15) >> 2, p = lane & 3;
  const unsigned lb = lds_addr(smem);
  unsigned roff[4];
#pragma unroll
  for (int c = 0; c < 2; ++c) {
    roff[c] = lb + kg * 4096 + (8 * hh + q) * 256 + (4 * ((2 * wf + c) ^ q) + 2 * blk + (p >> 1)) * 16 + 8 * (p & 1);
    roff[2 + c] = lb + kg * 4096 + 16384 + (8 * hh + q) * 256 + (4 * ((2 * wl + (c ^ wf)) ^ q) + 2 * blk + (p >> 1)) * 16 + 8 * (p & 1);
  }
  const bf16x2 ones = {(__bf16)1.0f, (__bf16)1.0f};
  u32x2 Rr[2][2][4][2];
  WAIT_V_BAR(12);
  STAMP(2, 1);
#pragma unroll
  for (int ks = 0; ks < 2; ++ks)
#pragma unroll
    for (int o = 0; o < 4; ++o) { DS_TR(Rr[0][ks][o][0], roff[o], ks * 8192); DS_TR(Rr[0][ks][o][1], roff[o], ks * 8192 + 1024); }
#pragma unroll
  for (int c = 0; c < 7; ++c) {
    const int set = c & 1;
    if (c < 6) {
      if (c < 4) { WAIT_V_BAR(8); } else if (c == 4) { WAIT_V_BAR(4); } else { WAIT_V_BAR(0); }
      if (c + 4 < 7) stage(c + 4, c & 3);
    } else {
      WAIT_L0();
    }
#pragma unroll
    for (int ks = 0; ks < 2; ++ks)
#pragma unroll
      for (int o = 0; o < 4; o += 2)
        asm volatile("" : "+v"(Rr[set][ks][o][0]), "+v"(Rr[set][ks][o][1]), "+v"(Rr[set][ks][o + 1][0]), "+v"(Rr[set][ks][o + 1][1]));
    unsigned ra[4];
#pragma unroll
    for (int o = 0; o < 4; ++o) ra[o] = roff[o] + ((c + 1) & 3) * K3_CHUNK;
    __builtin_amdgcn_sched_barrier(0);
#pragma unroll
    for (int ks = 0; ks < 2; ++ks) {
      bf16x8 fr[4];
#pragma unroll
      for (int o = 0; o < 4; ++o) fr[o] = cat_frag(Rr[set][ks][o][0], Rr[set][ks][o][1]);
#pragma unroll
      for (int a = 0; a < 2; ++a)
#pragma unroll
        for (int b = 0; b < 2; ++b) {
          acc[a][b] = __builtin_amdgcn_mfma_f32_32x32x16_bf16(fr[a], fr[2 + b], acc[a][b], 0, 0, 0);
          const int o = a * 2 + b;
          if (c < 6) { DS_TR(Rr[set ^ 1][ks][o][0], ra[o], ks * 8192); DS_TR(Rr[set ^ 1][ks][o][1], ra[o], ks * 8192 + 1024); }
          bf16x2 pr = {fr[2][2 * o], fr[2][2 * o + 1]};
          sm = __builtin_amdgcn_fdot2_f32_bf16(pr, ones, sm, false);
          sq = __builtin_amdgcn_fdot2_f32_bf16(pr, pr, sq, false);
          __builtin_amdgcn_sched_barrier(0);
        }
    }
  }
  STAMP(2, 2);
  {
    sm += __shfl_xor(sm, 32);
    sq += __shfl_xor(sq, 32);
    f32x2* st = (f32x2*)(smem + K3_XOFF + 1024);
    if (lane < 32) st[((kg * 2 + wl) * 2 + wf) * 32 + lane] = (f32x2){sm, sq};
  }
  asm volatile("s_waitcnt lgkmcnt(0)\n\ts_barrier" ::: "memory");
  {
    f32x4* xb = (f32x4*)(smem + wv * 8192) + lane;
#pragma unroll
    for (int b = 0; b < 2; ++b)
#pragma unroll
      for (int g = 0; g < 4; ++g) {
        const f32x16& t = kg ? acc[0][b] : acc[1][b];
        xb[(b * 4 + g) * 64] = (f32x4){t[4 * g], t[4 * g + 1], t[4 * g + 2], t[4 * g + 3]};
      }
  }
  __syncthreads();
  f32x16 av[2];
  {
    const f32x4* xb = (const f32x4*)(smem + (wv ^ 4) * 8192) + lane;
#pragma unroll
    for (int b = 0; b < 2; ++b) {
      av[b] = kg ? acc[1][b] : acc[0][b];
#pragma unroll
      for (int g = 0; g < 4; ++g) {
        f32x4 t = xb[(b * 4 + g) * 64];
#pragma unroll
        for (int e = 0; e < 4; ++e) av[b][4 * g + e] += t[e];
      }
    }
  }
  float mB[2], rB[2];
  {
    const f32x2* st = (const f32x2*)(smem + K3_XOFF + 1024);
#pragma unroll
    for (int b = 0; b < 2; ++b) {
      f32x2 s0 = st[((0 * 2 + wl) * 2 + (b ^ wf)) * 32 + (lane & 31)];
      f32x2 s1 = st[((1 * 2 + wl) * 2 + (b ^ wf)) * 32 + (lane & 31)];
      const float mean = (s0.x + s1.x) * (1.f / DD);
      const float var = fmaxf((s0.y + s1.y) * (1.f / DD) - mean * mean, 0.f);
      mB[b] = bfr(mean);
      rB[b] = 1.f / bfr(sqrtf(bfr(bfr(var) + 1e-5f)));
    }
  }
  const float* Ss = (const float*)(smem + K3_XOFF);
  const unsigned short* Bs = (const unsigned short*)(smem + K3_XOFF + 512);
  {
    const int fl = wf * 64 + kg * 32;
    f32x2 Sv[8], Bv[8];
#pragma unroll
    for (int g = 0; g < 4; ++g) {
      f32x4 s4 = *(const f32x4*)(Ss + fl + 8 * g + 4 * hh);
      u32x2 b2 = *(const u32x2*)(Bs + fl + 8 * g + 4 * hh);
      Sv[2 * g] = (f32x2){s4[0], s4[1]}; Sv[2 * g + 1] = (f32x2){s4[2], s4[3]};
      Bv[2 * g] = (f32x2){bflo(b2[0]), bfhi(b2[0])}; Bv[2 * g + 1] = (f32x2){bflo(b2[1]), bfhi(b2[1])};
    }
    const float c0 = -2.f * 0.7978845608028654f * 1.4426950408889634f;
    const float c1 = c0 * 0.044715f;
#pragma unroll
    for (int b = 0; b < 2; ++b) {
      const f32x2 nm = {-mB[b], -mB[b]}, rr = {rB[b], rB[b]};
      f32x2 v[8], t[8];
#pragma unroll
      for (int e = 0; e < 8; ++e) {
        f32x2 a2 = {av[b][2 * e], av[b][2 * e + 1]};
        v[e] = __builtin_elementwise_fma(__builtin_elementwise_fma(nm, Sv[e], a2), rr, Bv[e]);
      }
#pragma unroll
      for (int e = 0; e < 8; ++e) t[e] = __builtin_elementwise_fma(v[e] * v[e], (f32x2){c1, c1}, (f32x2){c0, c0});
#pragma unroll
      for (int e = 0; e < 8; ++e) t[e] = v[e] * t[e];
      __builtin_amdgcn_sched_barrier(0);
#pragma unroll
      for (int e = 0; e < 8; ++e) t[e] = (f32x2){__builtin_amdgcn_exp2f(t[e].x), __builtin_amdgcn_exp2f(t[e].y)};
#pragma unroll
      for (int e = 0; e < 8; ++e) t[e] = t[e] + 1.f;
      __builtin_amdgcn_sched_barrier(0);
#pragma unroll
      for (int e = 0; e < 8; ++e) t[e] = (f32x2){__builtin_amdgcn_rcpf(t[e].x), __builtin_amdgcn_rcpf(t[e].y)};
      unsigned o[8];
#pragma unroll
      for (int e = 0; e < 8; ++e) { f32x2 g = v[e] * t[e]; o[e] = pack2(g.x, g.y); }
      const int ltile = (l0 + wl * 64 + (b ^ wf) * 32) >> 5;
      const int fstep = (f0 + fl) >> 4;
#pragma unroll
      for (int s = 0; s < 2; ++s) {
        u32x4 ov = {o[4 * s], o[4 * s + 1], o[4 * s + 2], o[4 * s + 3]};
        *(u32x4*)(Hf + (((size_t)ltile * (FF / 16) + fstep + s) * 64 + lane) * 8) = ov;
      }
    }
  }
  STAMP(2, 3);
}

#define K4_NBUF 4
#define K4_CHUNK 32768
__global__ __launch_bounds__(512) void k4_kernel(
    const unsigned short* __restrict__ Hf, const unsigned short* __restrict__ w2,
    const unsigned short* __restrict__ b2, const unsigned short* __restrict__ hT,
    unsigned short* __restrict__ out) {
  extern __shared__ __attribute__((aligned(16))) char smem[];
  const int tid = threadIdx.x, lane = tid & 63, wv = __builtin_amdgcn_readfirstlane(tid >> 6);
  const int bid = blockIdx.x;
  const int xcd = bid & 7, idx = bid >> 3;
  const int lt = 4 * xcd + (idx / 7), dt = idx % 7;
  const int l0 = lt * 64, d0 = dt * 64;
  const int kg = wv >> 2;
  const int wl = (wv >> 1) & 1, wd = wv & 1;
  const int hh = lane >> 5, blk = (lane >> 4) & 1, q = (lane & 15) >> 2, p = lane & 3;
  STAMP(3, 0);
  const int lrow = l0 + wl * 32 + (lane & 31);
  const int dbase = d0 + wd * 32 + 4 * hh + 16 * kg;
  unsigned short hres[8];
  u32x2 bres[2];
#pragma unroll
  for (int e = 0; e < 8; ++e) hres[e] = hT[(size_t)(dbase + (e & 3) + 8 * (e >> 2)) * LL + lrow];
#pragma unroll
  for (int g = 0; g < 2; ++g) bres[g] = *(const u32x2*)(b2 + dbase + 8 * g);

  const int srow = lane >> 3, sch = lane & 7;
  const unsigned short* hsrc = Hf + (((size_t)(l0 >> 5) * (FF / 16) + wv) * 64 + lane) * 8;
  const unsigned short* wsrc = w2 + (size_t)(wv * 8 + srow) * DD + d0 + (sch ^ (((srow >> 1) & 1) << 2)) * 8;
  auto stage = [&](int c, int buf) {
    char* base = smem + buf * K4_CHUNK;
#pragma unroll
    for (int i = 0; i < 2; ++i) {
      const int piece = i * 8 + wv;
      __builtin_amdgcn_global_load_lds((const unsigned*)(hsrc + ((size_t)i * (FF / 16) + c * 8) * 512),
                                       (unsigned*)(base + piece * 1024), 16, 0, 0);
      __builtin_amdgcn_global_load_lds((const unsigned*)(wsrc + (size_t)(c * 128 + i * 64) * DD),
                                       (unsigned*)(base + 16384 + piece * 1024), 16, 0, 0);
    }
  };
  stage(0, 0); stage(1, 1); stage(2, 2); stage(3, 3);
  f32x16 acc;
#pragma unroll
  for (int e = 0; e < 16; ++e) acc[e] = 0.f;
  const unsigned lb = lds_addr(smem);
  const unsigned boff = lb + kg * 2048 + 16384 + (4 * hh + q) * 128 + ((4 * wd + 2 * blk + (p >> 1)) ^ (((q >> 1) & 1) << 2)) * 16 + 8 * (p & 1);
  const unsigned aoff = lb + kg * 1024 + wl * 8192 + lane * 16;
  u32x4 Hr[2][4];
  u32x2 Wr[2][4][2];
  WAIT_V_BAR(12);
  STAMP(3, 1);
#pragma unroll
  for (int ks = 0; ks < 4; ++ks) {
    DS_B128(Hr[0][ks], aoff, ks * 2048);
    DS_TR(Wr[0][ks][0], boff, ks * 4096);
    DS_TR(Wr[0][ks][1], boff, ks * 4096 + 1024);
  }
#pragma unroll
  for (int c = 0; c < 14; ++c) {
    const int set = c & 1;
    if (c < 13) {
      if (c < 11) { WAIT_V_BAR(8); } else if (c == 11) { WAIT_V_BAR(4); } else { WAIT_V_BAR(0); }
      if (c + 4 < 14) stage(c + 4, c & 3);
    } else {
      WAIT_L0();
    }
#pragma unroll
    for (int ks = 0; ks < 4; ++ks) asm volatile("" : "+v"(Hr[set][ks]), "+v"(Wr[set][ks][0]), "+v"(Wr[set][ks][1]));
    const unsigned aa = aoff + ((c + 1) & 3) * K4_CHUNK, bb = boff + ((c + 1) & 3) * K4_CHUNK;
    __builtin_amdgcn_sched_barrier(0);
#pragma unroll
    for (int ks = 0; ks < 4; ++ks) {
      bf16x8 hf = __builtin_bit_cast(bf16x8, Hr[set][ks]);
      bf16x8 wfr = cat_frag(Wr[set][ks][0], Wr[set][ks][1]);
      acc = __builtin_amdgcn_mfma_f32_32x32x16_bf16(wfr, hf, acc, 0, 0, 0);
      if (c < 13) {
        DS_B128(Hr[set ^ 1][ks], aa, ks * 2048);
        DS_TR(Wr[set ^ 1][ks][0], bb, ks * 4096);
        DS_TR(Wr[set ^ 1][ks][1], bb, ks * 4096 + 1024);
      }
      __builtin_amdgcn_sched_barrier(0);
    }
  }
  STAMP(3, 2);
  asm volatile("s_waitcnt lgkmcnt(0)\n\ts_barrier" ::: "memory");
  {
    f32x4* xb = (f32x4*)(smem + wv * 2048) + lane;
#pragma unroll
    for (int g = 0; g < 2; ++g)
      xb[g * 64] = kg ? (f32x4){acc[4 * g], acc[4 * g + 1], acc[4 * g + 2], acc[4 * g + 3]}
                      : (f32x4){acc[8 + 4 * g], acc[9 + 4 * g], acc[10 + 4 * g], acc[11 + 4 * g]};
  }
  __syncthreads();
  {
    const f32x4* xb = (const f32x4*)(smem + (wv ^ 4) * 2048) + lane;
#pragma unroll
    for (int g = 0; g < 2; ++g) {
      f32x4 t = xb[g * 64];
      f32x4 m = kg ? (f32x4){acc[8 + 4 * g], acc[9 + 4 * g], acc[10 + 4 * g], acc[11 + 4 * g]}
                   : (f32x4){acc[4 * g], acc[4 * g + 1], acc[4 * g + 2], acc[4 * g + 3]};
      t += m;
      float bv[4] = {bflo(bres[g][0]), bfhi(bres[g][0]), bflo(bres[g][1]), bfhi(bres[g][1])};
      float o[4];
#pragma unroll
      for (int e = 0; e < 4; ++e) o[e] = bf2f(hres[4 * g + e]) + (t[e] + bv[e]);
      u32x2 ov = {pack2(o[0], o[1]), pack2(o[2], o[3])};
      *(u32x2*)(out + (size_t)lrow * DD + dbase + 8 * g) = ov;
    }
  }
  STAMP(3, 3);
}

extern "C" void kernel_launch(void* const* d_in, const int* in_sizes, int n_in, void* d_out,
                              int out_size, void* d_ws, size_t ws_size, hipStream_t stream) {
  const unsigned short* x = (const unsigned short*)d_in[0];
  const unsigned short* logA = (const unsigned short*)d_in[1];
  const unsigned short* Aim = (const unsigned short*)d_in[2];
  const unsigned short* Cre = (const unsigned short*)d_in[3];
  const unsigned short* Cim = (const unsigned short*)d_in[4];
  const unsigned short* logdt = (const unsigned short*)d_in[5];
  const unsigned short* Dskip = (const unsigned short*)d_in[6];
  const unsigned short* w1 = (const unsigned short*)d_in[8];
  const unsigned short* b1 = (const unsigned short*)d_in[9];
  const unsigned short* w2 = (const unsigned short*)d_in[10];
  const unsigned short* b2 = (const unsigned short*)d_in[11];
  unsigned short* out = (unsigned short*)d_out;

  char* ws = (char*)d_ws;
  const size_t T = (size_t)DD * LL * 2;
  unsigned short* uT = (unsigned short*)(ws);
  unsigned short* xT = (unsigned short*)(ws + T);
  unsigned short* Kx = (unsigned short*)(ws + 2 * T);
  unsigned short* hT = (unsigned short*)(ws + 3 * T);
  float* S1 = (float*)(ws + 4 * T);
  unsigned short* Hf = (unsigned short*)(ws + 4 * T + 8192);

  (void)hipFuncSetAttribute((const void*)k3_kernel, hipFuncAttributeMaxDynamicSharedMemorySize, K3_LDS);
  (void)hipFuncSetAttribute((const void*)k4_kernel, hipFuncAttributeMaxDynamicSharedMemorySize, K4_NBUF * K4_CHUNK);

  k1_kernel<<<K1_KG_BLOCKS + K1_LN_BLOCKS + K1_CS_BLOCKS + K1_PF_BLOCKS, 256, 0, stream>>>(x, logA, Aim, Cre, Cim, logdt, w1, w2, uT, xT, Kx, S1);
  k2_kernel<<<DD, 256, 0, stream>>>(uT, xT, Kx, Dskip, hT);
  k3_kernel<<<224, 512, K3_LDS, stream>>>(hT, w1, S1, b1, Hf);
  k4_kernel<<<224, 512, K4_NBUF * K4_CHUNK, stream>>>(Hf, w2, b2, hT, out);
}
```

```cpp
#include <hip/hip_runtime.h>
#include <hip/hip_bf16.h>
#include <stdint.h>

#define LL 2048
#define DD 448
#define NN 256
#define FF 1792

typedef __bf16 bf16x2 __attribute__((ext_vector_type(2)));
typedef __bf16 bf16x8 __attribute__((ext_vector_type(8)));
typedef _Float16 f16x2 __attribute__((ext_vector_type(2)));
typedef _Float16 f16x8 __attribute__((ext_vector_type(8)));
typedef float f32x2 __attribute__((ext_vector_type(2)));
typedef float f32x4 __attribute__((ext_vector_type(4)));
typedef float f32x16 __attribute__((ext_vector_type(16)));
typedef unsigned u32x4 __attribute__((ext_vector_type(4)));
typedef unsigned u32x2 __attribute__((ext_vector_type(2)));

#define DI __device__ __forceinline__

#define STAMP(k, i)

DI float bfr(float x) { return (float)(__bf16)x; }
DI float bf2f(unsigned short v) { return __uint_as_float(((unsigned)v) << 16); }
DI float bflo(unsigned w) { return __uint_as_float(w << 16); }
DI float bfhi(unsigned w) { return __uint_as_float(w & 0xffff0000u); }
DI unsigned pack2(float a, float b) {
  bf16x2 r = {(__bf16)a, (__bf16)b};
  return __builtin_bit_cast(unsigned, r);
}
DI unsigned packh(f32x2 a) {
  f16x2 r = {(_Float16)a.x, (_Float16)a.y};
  return __builtin_bit_cast(unsigned, r);
}
DI unsigned lds_addr(const void* p) {
  return (unsigned)(uintptr_t)(__attribute__((address_space(3))) const char*)p;
}

#define WAIT_V_BAR(n) asm volatile("s_waitcnt vmcnt(" #n ") lgkmcnt(0)\n\ts_barrier" ::: "memory")
#define WAIT_L0() asm volatile("s_waitcnt lgkmcnt(0)" ::: "memory")
#define DS_TR(dst, addr, off) asm volatile("ds_read_b64_tr_b16 %0, %1 offset:%2" : "=v"(dst) : "v"(addr), "i"(off))
#define DS_B128(dst, addr, off) asm volatile("ds_read_b128 %0, %1 offset:%2" : "=v"(dst) : "v"(addr), "i"(off))

DI bf16x8 cat_frag(u32x2 lo, u32x2 hi) {
  u32x4 v = __builtin_shufflevector(lo, hi, 0, 1, 2, 3);
  return __builtin_bit_cast(bf16x8, v);
}

#define K1_LN_BLOCKS 64
#define K1_KG_BLOCKS DD
#define K1_ROWB 272
#define K1_WREG (48 * K1_ROWB)
#define K1_LNROW 928

DI f32x2 cexp_p(float lr2, float lih, float lil, float p) {
  float mag = __builtin_amdgcn_exp2f(lr2 * p);
  float ph = lih * p;
  float e = __builtin_fmaf(lih, p, -ph);
  float fr = __builtin_amdgcn_fractf(ph) + __builtin_fmaf(lil, p, e);
  f32x2 cs = {__builtin_amdgcn_cosf(fr), __builtin_amdgcn_sinf(fr)};
  return cs * mag;
}
DI f32x2 crot(f32x2 w) { return (f32x2){-w.y, w.x}; }
DI f32x2 cmul(f32x2 a, f32x2 w, f32x2 wr) { return __builtin_elementwise_fma(a.yy, wr, a.xx * w); }
DI int cidx_of_lag(int lag) {
  __bf16 b = (__bf16)(float)lag;
  int bits = (int)__builtin_bit_cast(unsigned short, b);
  return lag < 128 ? lag : bits - (0x4300 - 128);
}
DI float sum8(float v) {
  v += __builtin_bit_cast(float, __builtin_amdgcn_mov_dpp(__builtin_bit_cast(int, v), 0xB1, 0xF, 0xF, true));
  v += __builtin_bit_cast(float, __builtin_amdgcn_mov_dpp(__builtin_bit_cast(int, v), 0x4E, 0xF, 0xF, true));
  v += __builtin_bit_cast(float, __builtin_amdgcn_mov_dpp(__builtin_bit_cast(int, v), 0x141, 0xF, 0xF, true));
  return v;
}
typedef short s16x4 __attribute__((ext_vector_type(4)));
typedef __attribute__((address_space(3))) s16x4* lds_s16x4_ptr;

__global__ __launch_bounds__(256) void k1_kernel(
    const unsigned short* __restrict__ x, const unsigned short* __restrict__ logA,
    const unsigned short* __restrict__ Aim, const unsigned short* __restrict__ Cre,
    const unsigned short* __restrict__ Cim, const unsigned short* __restrict__ logdt,
    const unsigned short* __restrict__ w1, const unsigned short* __restrict__ w2,
    unsigned short* __restrict__ uT, unsigned short* __restrict__ xT,
    unsigned short* __restrict__ Kx, unsigned* __restrict__ flags) {
  __shared__ __attribute__((aligned(16))) char smem[4 * K1_WREG];
  const int tid = threadIdx.x, lane = tid & 63, wv = __builtin_amdgcn_readfirstlane(tid >> 6);
  const int bid = blockIdx.x;
  STAMP(0, 0);
  if (bid == 0 && tid < 16) flags[tid * 64] = 0u;
  if (bid < K1_LN_BLOCKS) {
    const int l0 = bid * 32;
    const int r = lane >> 3, c = lane & 7;
    const int row = wv * 8 + r;
    u32x4 raw[7];
#pragma unroll
    for (int i = 0; i < 7; ++i) raw[i] = *(const u32x4*)(x + (size_t)(l0 + row) * DD + (c + 8 * i) * 8);
    f32x2 s2 = {0.f, 0.f};
#pragma unroll
    for (int i = 0; i < 7; ++i)
#pragma unroll
      for (int e = 0; e < 4; ++e) s2 += (f32x2){bflo(raw[i][e]), bfhi(raw[i][e])};
    const float mean = sum8(s2.x + s2.y) * (1.f / DD);
    f32x2 q2 = {0.f, 0.f};
    const f32x2 mm = {mean, mean};
#pragma unroll
    for (int i = 0; i < 7; ++i)
#pragma unroll
      for (int e = 0; e < 4; ++e) { f32x2 t = (f32x2){bflo(raw[i][e]), bfhi(raw[i][e])} - mm; q2 = __builtin_elementwise_fma(t, t, q2); }
    const float var = sum8(q2.x + q2.y) * (1.f / DD);
    const float m = bfr(mean);
    const float sd = bfr(sqrtf(bfr(bfr(var) + 1e-5f)));
    const float rs = 1.f / sd;
    const f32x2 m2 = {m, m}, rs2 = {rs, rs}, nsd2 = {-sd, -sd};
    char* tu = smem;
#pragma unroll
    for (int i = 0; i < 7; ++i) {
      u32x4 o;
#pragma unroll
      for (int e = 0; e < 4; ++e) {
        f32x2 t = (f32x2){bflo(raw[i][e]), bfhi(raw[i][e])} - m2;
        unsigned tb = pack2(t.x, t.y);
        f32x2 dd = {bflo(tb), bfhi(tb)};
        f32x2 qv = dd * rs2;
        f32x2 er = __builtin_elementwise_fma(qv, nsd2, dd);
        qv = __builtin_elementwise_fma(er, rs2, qv);
        o[e] = pack2(qv.x, qv.y);
      }
      *(u32x4*)(tu + row * K1_LNROW + (c + 8 * i) * 16) = o;
    }
    __syncthreads();
    const int g = lane >> 4, q = (lane & 15) >> 2, pp = lane & 3;
    const char* rb = tu + (8 * g + q) * K1_LNROW + pp * 8;
#pragma unroll
    for (int it = 0; it < 7; ++it) {
      const int c0 = (it * 4 + wv) * 16;
      s16x4 a = __builtin_amdgcn_ds_read_tr16_b64_v4i16((lds_s16x4_ptr)(rb + c0 * 2));
      s16x4 b = __builtin_amdgcn_ds_read_tr16_b64_v4i16((lds_s16x4_ptr)(rb + 4 * K1_LNROW + c0 * 2));
      u32x2 a2 = __builtin_bit_cast(u32x2, a), b2 = __builtin_bit_cast(u32x2, b);
      u32x4 o = {a2[0], a2[1], b2[0], b2[1]};
      *(u32x4*)(uT + (size_t)(c0 + (lane & 15)) * LL + l0 + 8 * g) = o;
    }
    __syncthreads();
#pragma unroll
    for (int i = 0; i < 7; ++i) *(u32x4*)(tu + row * K1_LNROW + (c + 8 * i) * 16) = raw[i];
    __syncthreads();
#pragma unroll
    for (int it = 0; it < 7; ++it) {
      const int c0 = (it * 4 + wv) * 16;
      s16x4 a = __builtin_amdgcn_ds_read_tr16_b64_v4i16((lds_s16x4_ptr)(rb + c0 * 2));
      s16x4 b = __builtin_amdgcn_ds_read_tr16_b64_v4i16((lds_s16x4_ptr)(rb + 4 * K1_LNROW + c0 * 2));
      u32x2 a2 = __builtin_bit_cast(u32x2, a), b2 = __builtin_bit_cast(u32x2, b);
      u32x4 o = {a2[0], a2[1], b2[0], b2[1]};
      *(u32x4*)(xT + (size_t)(c0 + (lane & 15)) * LL + l0 + 8 * g) = o;
    }
  } else {
    const int d = bid - K1_LN_BLOCKS;
    const int nb = d * NN + tid;
    float lr2, lih, lil, cr, ci;
    u32x4 pf1, pf2;
    {
      const unsigned short la = logA[nb], ai = Aim[nb], cre = Cre[nb], cim = Cim[nb];
      const unsigned short ldt = logdt[d];
      const float dt = bfr(expf(bf2f(ldt)));
      const float I2PH = 0.15915494f;
      const float I2PL = 6.4206382e-09f;
      const float lr = -bfr(expf(bf2f(la))) * dt;
      const float li = bf2f(ai) * dt;
      lr2 = lr * 1.44269504f;
      lih = li * I2PH;
      lil = __builtin_fmaf(li, I2PH, -lih) + li * I2PL;
      cr = 2.f * bf2f(cre) * dt;
      ci = 2.f * bf2f(cim) * dt;
    }
    float cm = fmaxf(fabsf(cr), fabsf(ci));
#pragma unroll
    for (int o = 32; o > 0; o >>= 1) cm = fmaxf(cm, __shfl_xor(cm, o));
    const float scale = cm > 0.f ? cm : 1.f;
    const float inv = 1.f / scale;
    STAMP(0, 1);
    {
      const int pi = d * 224 + (tid < 224 ? tid : 223);
      asm volatile("global_load_dwordx4 %0, %1, off" : "=v"(pf1) : "v"((const u32x4*)w1 + pi) : "memory");
      asm volatile("global_load_dwordx4 %0, %1, off" : "=v"(pf2) : "v"((const u32x4*)w2 + pi) : "memory");
    }
    char* reg = smem + wv * K1_WREG;
    unsigned* wr_ = (unsigned*)(reg + lane * 4);
#define KROW(r) wr_[(r) * (K1_ROWB / 4)]
    const int i16 = lane & 15, kg = lane >> 4;
    const char* rdA = reg + i16 * K1_ROWB + kg * 16;
    f32x4 acc0 = {0, 0, 0, 0}, acc1 = {0, 0, 0, 0}, acc2 = {0, 0, 0, 0};
    {
      const f32x2 E1 = cexp_p(lr2, lih, lil, 1.f), E8 = cexp_p(lr2, lih, lil, 8.f), E128 = cexp_p(lr2, lih, lil, 128.f);
      const f32x2 E256 = cexp_p(lr2, lih, lil, 256.f), E512 = cexp_p(lr2, lih, lil, 512.f), E1024 = cexp_p(lr2, lih, lil, 1024.f);
      const f32x2 E1r = crot(E1), E128r = crot(E128), E256r = crot(E256), E512r = crot(E512);
      f32x2 a[16];
      {
        const f32x2 w = {E8.x, -E8.y}, wr = crot(w);
        const f32x2 w2 = cmul(w, w, wr), w2r = crot(w2);
        const f32x2 w4 = cmul(w2, w2, w2r), w4r = crot(w4);
        a[0] = (f32x2){cr * inv, -ci * inv};
        a[1] = cmul(a[0], w, wr);
        a[2] = cmul(a[0], w2, w2r);
        a[3] = cmul(a[1], w2, w2r);
#pragma unroll
        for (int i = 4; i < 16; ++i) a[i] = cmul(a[i - 4], w4, w4r);
      }
      f32x2 e[8];
      e[0] = (f32x2){1.f, 0.f}; e[1] = E1;
      e[2] = cmul(E1, E1, E1r);
      e[3] = cmul(e[2], E1, E1r);
      const f32x2 e2r = crot(e[2]);
      e[4] = cmul(e[2], e[2], e2r);
      const f32x2 e4r = crot(e[4]);
      e[5] = cmul(e[1], e[4], e4r); e[6] = cmul(e[2], e[4], e4r); e[7] = cmul(e[3], e[4], e4r);
#pragma unroll
      for (int i = 0; i < 16; ++i) KROW(i) = packh(a[i]);
#pragma unroll
      for (int j = 0; j < 8; ++j) { KROW(16 + j) = packh(e[j]); KROW(24 + j) = packh(j ? cmul(e[j], E128, E128r) : E128); }
      const f32x2 E384 = cmul(E256, E128, E128r), E384r = crot(E384);
#pragma unroll
      for (int r = 0; r < 4; ++r) {
        KROW(32 + r) = packh(r ? cmul(e[2 * r], E256, E256r) : E256);
        KROW(36 + r) = packh(r ? cmul(e[2 * r], E384, E384r) : E384);
      }
      f32x2 u[4];
      u[0] = E512; u[1] = cmul(E512, E128, E128r); u[2] = cmul(E512, E256, E256r); u[3] = cmul(E512, E384, E384r);
#pragma unroll
      for (int w = 0; w < 4; ++w) { KROW(40 + 2 * w) = packh(u[w]); KROW(41 + 2 * w) = packh(cmul(u[w], e[4], e4r)); }
      f32x2 v[9];
      v[0] = E1024; v[1] = cmul(E1024, E128, E128r); v[2] = cmul(E1024, E256, E256r); v[3] = cmul(E1024, E384, E384r);
#pragma unroll
      for (int c = 4; c < 9; ++c) v[c] = cmul(v[c - 4], E512, E512r);
      unsigned vp[9];
#pragma unroll
      for (int c = 0; c < 9; ++c) vp[c] = packh(v[c]);
      __builtin_amdgcn_fence(__ATOMIC_RELEASE, "wavefront");
      __builtin_amdgcn_wave_barrier();
      __builtin_amdgcn_fence(__ATOMIC_ACQUIRE, "wavefront");
      f16x8 af[4];
#pragma unroll
      for (int s = 0; s < 4; ++s) {
        af[s] = *(const f16x8*)(rdA + 64 * s);
        f16x8 b0 = *(const f16x8*)(rdA + 16 * K1_ROWB + 64 * s);
        f16x8 b1 = *(const f16x8*)(rdA + 32 * K1_ROWB + 64 * s);
        acc0 = __builtin_amdgcn_mfma_f32_16x16x32_f16(af[s], b0, acc0, 0, 0, 0);
        acc1 = __builtin_amdgcn_mfma_f32_16x16x32_f16(af[s], b1, acc1, 0, 0, 0);
      }
      __builtin_amdgcn_fence(__ATOMIC_RELEASE, "wavefront");
      __builtin_amdgcn_wave_barrier();
      __builtin_amdgcn_fence(__ATOMIC_ACQUIRE, "wavefront");
#pragma unroll
      for (int c = 0; c < 9; ++c) KROW(16 + c) = vp[c];
      __builtin_amdgcn_fence(__ATOMIC_RELEASE, "wavefront");
      __builtin_amdgcn_wave_barrier();
      __builtin_amdgcn_fence(__ATOMIC_ACQUIRE, "wavefront");
#pragma unroll
      for (int s = 0; s < 4; ++s) {
        f16x8 b2 = *(const f16x8*)(rdA + 16 * K1_ROWB + 64 * s);
        acc2 = __builtin_amdgcn_mfma_f32_16x16x32_f16(af[s], b2, acc2, 0, 0, 0);
      }
    }
#undef KROW
    acc0 *= scale; acc1 *= scale; acc2 *= scale;
    __syncthreads();
    STAMP(0, 2);
    float* red = (float*)smem;
    unsigned short* kd = (unsigned short*)(smem + 12288);
    *(f32x4*)(red + ((wv * 3 + 0) * 64 + lane) * 4) = acc0;
    *(f32x4*)(red + ((wv * 3 + 1) * 64 + lane) * 4) = acc1;
    *(f32x4*)(red + ((wv * 3 + 2) * 64 + lane) * 4) = acc2;
    __syncthreads();
    if (wv < 3) {
      f32x4 t = *(const f32x4*)(red + ((0 * 3 + wv) * 64 + lane) * 4);
#pragma unroll
      for (int w = 1; w < 4; ++w) t += *(const f32x4*)(red + ((w * 3 + wv) * 64 + lane) * 4);
      const int j = i16;
      const int lo0 = 128 * (j >> 3) + (j & 7);
      const int lo1 = j < 8 ? 256 + 128 * (j >> 2) + 2 * (j & 3) : 512 + 128 * ((j - 8) >> 1) + 4 * ((j - 8) & 1);
      const int lo2 = 1024 + 128 * j;
      const int lo = wv == 0 ? lo0 : (wv == 1 ? lo1 : lo2);
#pragma unroll
      for (int r = 0; r < 4; ++r) {
        const int pos = 8 * (4 * kg + r) + lo;
        __bf16 v0 = (__bf16)t[r];
        if (pos <= 2048) kd[cidx_of_lag(pos)] = __builtin_bit_cast(unsigned short, v0);
      }
    }
    __syncthreads();
    {
      unsigned o[4];
#pragma unroll
      for (int e = 0; e < 4; ++e) {
        const int lag = 2047 - (8 * tid + 2 * e);
        unsigned a = kd[cidx_of_lag(lag)], b = kd[cidx_of_lag(lag - 1)];
        o[e] = a | (b << 16);
      }
      u32x4 o0 = {o[0], o[1], o[2], o[3]};
      *(u32x4*)(Kx + (size_t)d * LL + 8 * tid) = o0;
    }
    asm volatile("" ::"v"(pf1), "v"(pf2));
  }
  STAMP(0, 3);
}

#define K2_CSTRIDE 4160
#define K2_CS_BLOCKS 28
#define K2_PK_BLOCKS 56
#define K2_AUX (K2_CS_BLOCKS + K2_PK_BLOCKS)
__global__ __launch_bounds__(256) void k2_kernel(
    const unsigned short* __restrict__ uT, const unsigned short* __restrict__ xT,
    const unsigned short* __restrict__ Kx, const unsigned short* __restrict__ Dskip,
    const unsigned short* __restrict__ w1, const unsigned short* __restrict__ w2,
    unsigned short* __restrict__ hT, float* __restrict__ S1, u32x4* __restrict__ W2f) {
  __shared__ __attribute__((aligned(16))) char smem[8 * K2_CSTRIDE + 4608];
  char* Kc = smem;
  char* Ul = smem + 8 * K2_CSTRIDE;
  const int tid = threadIdx.x, lane = tid & 63, wv = __builtin_amdgcn_readfirstlane(tid >> 6);
  STAMP(1, 0);
  if (blockIdx.x < K2_CS_BLOCKS) {
    const int f0 = blockIdx.x * 64;
    float* red = (float*)smem;
    const int fc = tid & 7, rg = tid >> 3;
    u32x4 raw[14];
#pragma unroll
    for (int r = 0; r < 14; ++r) raw[r] = *(const u32x4*)(w1 + (size_t)(rg * 14 + r) * FF + f0 + fc * 8);
    f32x2 s[4] = {{0.f, 0.f}, {0.f, 0.f}, {0.f, 0.f}, {0.f, 0.f}};
#pragma unroll
    for (int r = 0; r < 14; ++r)
#pragma unroll
      for (int e = 0; e < 4; ++e) s[e] += (f32x2){bflo(raw[r][e]), bfhi(raw[r][e])};
    *(f32x4*)(red + rg * 64 + fc * 8) = (f32x4){s[0].x, s[0].y, s[1].x, s[1].y};
    *(f32x4*)(red + rg * 64 + fc * 8 + 4) = (f32x4){s[2].x, s[2].y, s[3].x, s[3].y};
    __syncthreads();
    if (tid < 64) {
      float t = 0.f;
#pragma unroll
      for (int r = 0; r < 32; ++r) t += red[r * 64 + tid];
      S1[f0 + tid] = t;
    }
    STAMP(1, 3);
    return;
  }
  if (blockIdx.x < K2_AUX) {
    const int f0 = (blockIdx.x - K2_CS_BLOCKS) * 32;
    u32x4 raw[7];
#pragma unroll
    for (int i = 0; i < 7; ++i) { const int e = tid + 256 * i; raw[i] = *(const u32x4*)(w2 + (size_t)(f0 + e / 56) * DD + (e % 56) * 8); }
#pragma unroll
    for (int i = 0; i < 7; ++i) { const int e = tid + 256 * i; *(u32x4*)(smem + (e / 56) * K1_LNROW + (e % 56) * 16) = raw[i]; }
    __syncthreads();
    const int hh = lane >> 5, blk = (lane >> 4) & 1, q = (lane & 15) >> 2, pp = lane & 3;
#pragma unroll
    for (int i = 0; i < 7; ++i) {
      const int fr = i * 4 + wv;
      const int dg = fr >> 1, sl = fr & 1;
      const char* rb = smem + (16 * sl + 4 * hh + q) * K1_LNROW + (dg * 32 + 16 * blk + 4 * pp) * 2;
      s16x4 a = __builtin_amdgcn_ds_read_tr16_b64_v4i16((lds_s16x4_ptr)rb);
      s16x4 b = __builtin_amdgcn_ds_read_tr16_b64_v4i16((lds_s16x4_ptr)(rb + 8 * K1_LNROW));
      u32x2 a2 = __builtin_bit_cast(u32x2, a), b2 = __builtin_bit_cast(u32x2, b);
      const int st = (f0 >> 4) + sl;
      W2f[((size_t)(dg * 4 + (st & 3)) * 28 + (st >> 2)) * 64 + lane] = (u32x4){a2[0], a2[1], b2[0], b2[1]};
    }
    STAMP(1, 3);
    return;
  }
  const int d = blockIdx.x - K2_AUX;
  const int b = lane & 15, h = lane >> 4;
  u32x2 xx[2];
  float Dk;
  {
    const u32x4* kp = (const u32x4*)(Kx + (size_t)d * LL);
    u32x4 w0 = kp[tid];
    u32x4 w1 = {0, 0, 0, 0};
    if (tid < 255) w1 = kp[tid + 1];
    u32x4 uu = *(const u32x4*)(uT + (size_t)d * LL + tid * 8);
#pragma unroll
    for (int tt = 0; tt < 2; ++tt) xx[tt] = *(const u32x2*)(xT + (size_t)d * LL + 256 * (2 * wv + tt) + 16 * b + 4 * h);
    Dk = bf2f(Dskip[d]);
    unsigned w[8] = {w0[0], w0[1], w0[2], w0[3], w1[0], w1[1], w1[2], w1[3]};
#pragma unroll
    for (int m = 0; m < 4; ++m) {
      u32x4 ev = {w[m], w[m + 1], w[m + 2], w[m + 3]};
      u32x4 od;
#pragma unroll
      for (int i = 0; i < 4; ++i) od[i] = __builtin_amdgcn_alignbit(w[m + i + 1], w[m + i], 16);
      *(u32x4*)(Kc + (2 * m) * K2_CSTRIDE + tid * 16) = ev;
      *(u32x4*)(Kc + (2 * m + 1) * K2_CSTRIDE + tid * 16) = od;
    }
    u32x4 z = {0, 0, 0, 0};
    if (tid < 8) *(u32x4*)(Kc + tid * K2_CSTRIDE + 4096) = z;
    if (tid < 32) *(u32x4*)(Ul + tid * 16) = z;
    *(u32x4*)(Ul + 512 + tid * 16) = uu;
  }
  __syncthreads();
  STAMP(1, 1);
  f32x4 acc[8];
#pragma unroll
  for (int t = 0; t < 8; ++t) acc[t] = (f32x4){0, 0, 0, 0};
  const char* fbase = Kc + (7 - (b & 7)) * K2_CSTRIDE + 2 * (2040 - 8 * (b >> 3) + 8 * h);
  const char* gbase = Ul + 512 + 2 * (16 * b + 8 * h);
#pragma unroll
  for (int rr = 0; rr < 2; ++rr) {
    const int r = 4 * wv + 1 + 2 * rr;
    bf16x8 Fq[8], Gs[8];
#pragma unroll
    for (int q = 0; q < 8; ++q) Fq[q] = *(const bf16x8*)(fbase - 32 * (16 * q + r));
#pragma unroll
    for (int s = 0; s < 8; ++s) Gs[s] = *(const bf16x8*)(gbase + 32 * (16 * s - r));
#pragma unroll
    for (int q = 0; q < 8; ++q)
#pragma unroll
      for (int s = 0; s < 8 - q; ++s)
        acc[q + s] = __builtin_amdgcn_mfma_f32_16x16x32_bf16(Fq[q], Gs[s], acc[q + s], 0, 0, 0);
  }
  __syncthreads();
  STAMP(1, 2);
  float* red = (float*)smem;
#pragma unroll
  for (int t = 0; t < 8; ++t) *(f32x4*)(red + ((wv * 8 + t) * 64 + lane) * 4) = acc[t];
  __syncthreads();
#pragma unroll
  for (int tt = 0; tt < 2; ++tt) {
    const int t = 2 * wv + tt;
    f32x4 y = *(const f32x4*)(red + ((0 * 8 + t) * 64 + lane) * 4);
#pragma unroll
    for (int w = 1; w < 4; ++w) y += *(const f32x4*)(red + ((w * 8 + t) * 64 + lane) * 4);
    const int l = 256 * t + 16 * b + 4 * h;
    u32x2 uu = *(const u32x2*)(Ul + 512 + 2 * l);
    float u0 = bflo(uu[0]), u1 = bfhi(uu[0]), u2 = bflo(uu[1]), u3 = bfhi(uu[1]);
    float x0 = bflo(xx[tt][0]), x1 = bfhi(xx[tt][0]), x2 = bflo(xx[tt][1]), x3 = bfhi(xx[tt][1]);
    float h0 = x0 + bfr(bfr(y[0]) + bfr(u0 * Dk));
    float h1 = x1 + bfr(bfr(y[1]) + bfr(u1 * Dk));
    float h2 = x2 + bfr(bfr(y[2]) + bfr(u2 * Dk));
    float h3 = x3 + bfr(bfr(y[3]) + bfr(u3 * Dk));
    u32x2 o = {pack2(h0, h1), pack2(h2, h3)};
    *(u32x2*)(hT + (size_t)d * LL + l) = o;
  }
  STAMP(1, 3);
}

#define K3_NBUF 4
#define K3_CHUNK 32768
#define K3_XOFF (K3_NBUF * K3_CHUNK)
#define K3_LDS (K3_XOFF + 4096)
#define RAW_BAR() asm volatile("s_waitcnt lgkmcnt(0)\n\ts_barrier" ::: "memory")
__global__ __launch_bounds__(512) void k34_kernel(
    const unsigned short* __restrict__ hT, const unsigned short* __restrict__ w1,
    const float* __restrict__ S1, const unsigned short* __restrict__ b1,
    const u32x4* __restrict__ W2f, const unsigned short* __restrict__ b2,
    unsigned short* Hf, unsigned* flags, unsigned short* __restrict__ out) {
  extern __shared__ __attribute__((aligned(16))) char smem[];
  const int tid = threadIdx.x, lane = tid & 63, wv = __builtin_amdgcn_readfirstlane(tid >> 6);
  const int bid = blockIdx.x;
  const int xcd = bid & 7, idx = bid >> 3;
  const int lt = 2 * xcd + (idx / 14), ft = idx % 14;
  const int l0 = lt * 128, f0 = ft * 128;
  const int kg = wv >> 2;
  const int wf = (wv >> 1) & 1, wl = wv & 1;

  const int srow = lane >> 4, sch = lane & 15;
  const unsigned short* wsrc = w1 + (size_t)(wv * 4 + srow) * FF + f0 + (sch ^ ((srow & 3) << 2)) * 8;
  const unsigned short* hsrc = hT + (size_t)(wv * 4 + srow) * LL + l0 + (sch ^ ((srow & 3) << 2)) * 8;
  auto stage = [&](int c, int buf) {
    char* base = smem + buf * K3_CHUNK;
#pragma unroll
    for (int i = 0; i < 2; ++i) {
      const int k = c * 64 + i * 32;
      __builtin_amdgcn_global_load_lds((const unsigned*)(wsrc + (size_t)k * FF),
                                       (unsigned*)(base + (i * 8 + wv) * 1024), 16, 0, 0);
      __builtin_amdgcn_global_load_lds((const unsigned*)(hsrc + (size_t)k * LL),
                                       (unsigned*)(base + 16384 + (i * 8 + wv) * 1024), 16, 0, 0);
    }
  };
  STAMP(2, 0);
  const int lt2 = 4 * xcd + (idx / 7), dt2 = idx % 7;
  const int kq = wv >> 1, wd2 = wv & 1;
  const int lrow = lt2 * 64 + (kq >> 1) * 32 + (lane & 31);
  const int dbase = dt2 * 64 + wd2 * 32 + 4 * (lane >> 5) + 16 * (kq & 1);
  unsigned short hres[8];
  u32x2 bres[2];
#pragma unroll
  for (int e = 0; e < 8; ++e) hres[e] = hT[(size_t)(dbase + (e & 3) + 8 * (e >> 2)) * LL + lrow];
#pragma unroll
  for (int g = 0; g < 2; ++g) bres[g] = *(const u32x2*)(b2 + dbase + 8 * g);
  if (wv < 2) __builtin_amdgcn_global_load_lds((const unsigned*)(S1 + f0 + tid), (unsigned*)(smem + K3_XOFF + wv * 256), 4, 0, 0);
  else if (wv == 2) __builtin_amdgcn_global_load_lds((const unsigned*)(b1 + f0 + lane * 2), (unsigned*)(smem + K3_XOFF + 512), 4, 0, 0);
  stage(0, 0); stage(1, 1); stage(2, 2); stage(3, 3);

  f32x16 acc[2][2];
#pragma unroll
  for (int a = 0; a < 2; ++a)
#pragma unroll
    for (int b = 0; b < 2; ++b)
#pragma unroll
      for (int e = 0; e < 16; ++e) acc[a][b][e] = 0.f;
  float sm = 0.f, sq = 0.f;

  const int hh = lane >> 5, blk = (lane >> 4) & 1, q = (lane & 15) >> 2, p = lane & 3;
  const unsigned lb = lds_addr(smem);
  unsigned roff[4];
#pragma unroll
  for (int c = 0; c < 2; ++c) {
    roff[c] = lb + kg * 4096 + (8 * hh + q) * 256 + (4 * ((2 * wf + c) ^ q) + 2 * blk + (p >> 1)) * 16 + 8 * (p & 1);
    roff[2 + c] = lb + kg * 4096 + 16384 + (8 * hh + q) * 256 + (4 * ((2 * wl + (c ^ wf)) ^ q) + 2 * blk + (p >> 1)) * 16 + 8 * (p & 1);
  }
  const bf16x2 ones = {(__bf16)1.0f, (__bf16)1.0f};
  u32x2 Rr[2][2][4][2];
  u32x4 Wf[28];
  const u32x4* wp = W2f + ((size_t)((dt2 * 2 + wd2) * 4 + kq) * 28) * 64 + lane;
  WAIT_V_BAR(12);
  STAMP(2, 1);
#pragma unroll
  for (int ks = 0; ks < 2; ++ks)
#pragma unroll
    for (int o = 0; o < 4; ++o) { DS_TR(Rr[0][ks][o][0], roff[o], ks * 8192); DS_TR(Rr[0][ks][o][1], roff[o], ks * 8192 + 1024); }
#pragma unroll
  for (int c = 0; c < 7; ++c) {
    const int set = c & 1;
    if (c == 0) { WAIT_V_BAR(8); } else if (c == 1) { WAIT_V_BAR(12); } else if (c == 2) { WAIT_V_BAR(16); }
    else if (c == 3) { WAIT_V_BAR(20); } else if (c == 4) { WAIT_V_BAR(16); } else if (c == 5) { WAIT_V_BAR(12); }
    else { WAIT_L0(); }
    if (c + 4 < 7) stage(c + 4, c & 3);
    {
      const u32x4* wq = wp + c * 256;
      asm volatile("global_load_dwordx4 %0, %1, off" : "=v"(Wf[4 * c]) : "v"(wq) : "memory");
      asm volatile("global_load_dwordx4 %0, %1, off offset:1024" : "=v"(Wf[4 * c + 1]) : "v"(wq) : "memory");
      asm volatile("global_load_dwordx4 %0, %1, off offset:2048" : "=v"(Wf[4 * c + 2]) : "v"(wq) : "memory");
      asm volatile("global_load_dwordx4 %0, %1, off offset:3072" : "=v"(Wf[4 * c + 3]) : "v"(wq) : "memory");
    }
#pragma unroll
    for (int ks = 0; ks < 2; ++ks)
#pragma unroll
      for (int o = 0; o < 4; o += 2)
        asm volatile("" : "+v"(Rr[set][ks][o][0]), "+v"(Rr[set][ks][o][1]), "+v"(Rr[set][ks][o + 1][0]), "+v"(Rr[set][ks][o + 1][1]));
    unsigned ra[4];
#pragma unroll
    for (int o = 0; o < 4; ++o) ra[o] = roff[o] + ((c + 1) & 3) * K3_CHUNK;
    __builtin_amdgcn_sched_barrier(0);
#pragma unroll
    for (int ks = 0; ks < 2; ++ks) {
      bf16x8 fr[4];
#pragma unroll
      for (int o = 0; o < 4; ++o) fr[o] = cat_frag(Rr[set][ks][o][0], Rr[set][ks][o][1]);
#pragma unroll
      for (int a = 0; a < 2; ++a)
#pragma unroll
        for (int b = 0; b < 2; ++b) {
          acc[a][b] = __builtin_amdgcn_mfma_f32_32x32x16_bf16(fr[a], fr[2 + b], acc[a][b], 0, 0, 0);
          const int o = a * 2 + b;
          if (c < 6) { DS_TR(Rr[set ^ 1][ks][o][0], ra[o], ks * 8192); DS_TR(Rr[set ^ 1][ks][o][1], ra[o], ks * 8192 + 1024); }
          bf16x2 pr = {fr[2][2 * o], fr[2][2 * o + 1]};
          sm = __builtin_amdgcn_fdot2_f32_bf16(pr, ones, sm, false);
          sq = __builtin_amdgcn_fdot2_f32_bf16(pr, pr, sq, false);
          __builtin_amdgcn_sched_barrier(0);
        }
    }
  }
  STAMP(2, 2);
  {
    sm += __shfl_xor(sm, 32);
    sq += __shfl_xor(sq, 32);
    f32x2* st = (f32x2*)(smem + K3_XOFF + 1024);
    if (lane < 32) st[((kg * 2 + wl) * 2 + wf) * 32 + lane] = (f32x2){sm, sq};
  }
  asm volatile("s_waitcnt lgkmcnt(0)\n\ts_barrier" ::: "memory");
  {
    f32x4* xb = (f32x4*)(smem + wv * 8192) + lane;
#pragma unroll
    for (int b = 0; b < 2; ++b)
#pragma unroll
      for (int g = 0; g < 4; ++g) {
        const f32x16& t = kg ? acc[0][b] : acc[1][b];
        xb[(b * 4 + g) * 64] = (f32x4){t[4 * g], t[4 * g + 1], t[4 * g + 2], t[4 * g + 3]};
      }
  }
  RAW_BAR();
  f32x16 av[2];
  {
    const f32x4* xb = (const f32x4*)(smem + (wv ^ 4) * 8192) + lane;
#pragma unroll
    for (int b = 0; b < 2; ++b) {
      av[b] = kg ? acc[1][b] : acc[0][b];
#pragma unroll
      for (int g = 0; g < 4; ++g) {
        f32x4 t = xb[(b * 4 + g) * 64];
#pragma unroll
        for (int e = 0; e < 4; ++e) av[b][4 * g + e] += t[e];
      }
    }
  }
  float mB[2], rB[2];
  {
    const f32x2* st = (const f32x2*)(smem + K3_XOFF + 1024);
#pragma unroll
    for (int b = 0; b < 2; ++b) {
      f32x2 s0 = st[((0 * 2 + wl) * 2 + (b ^ wf)) * 32 + (lane & 31)];
      f32x2 s1 = st[((1 * 2 + wl) * 2 + (b ^ wf)) * 32 + (lane & 31)];
      const float mean = (s0.x + s1.x) * (1.f / DD);
      const float var = fmaxf((s0.y + s1.y) * (1.f / DD) - mean * mean, 0.f);
      mB[b] = bfr(mean);
      rB[b] = 1.f / bfr(sqrtf(bfr(bfr(var) + 1e-5f)));
    }
  }
  const float* Ss = (const float*)(smem + K3_XOFF);
  const unsigned short* Bs = (const unsigned short*)(smem + K3_XOFF + 512);
  {
    const int fl = wf * 64 + kg * 32;
    f32x2 Sv[8], Bv[8];
#pragma unroll
    for (int g = 0; g < 4; ++g) {
      f32x4 s4 = *(const f32x4*)(Ss + fl + 8 * g + 4 * hh);
      u32x2 b2 = *(const u32x2*)(Bs + fl + 8 * g + 4 * hh);
      Sv[2 * g] = (f32x2){s4[0], s4[1]}; Sv[2 * g + 1] = (f32x2){s4[2], s4[3]};
      Bv[2 * g] = (f32x2){bflo(b2[0]), bfhi(b2[0])}; Bv[2 * g + 1] = (f32x2){bflo(b2[1]), bfhi(b2[1])};
    }
    const float c0 = -2.f * 0.7978845608028654f * 1.4426950408889634f;
    const float c1 = c0 * 0.044715f;
#pragma unroll
    for (int b = 0; b < 2; ++b) {
      const f32x2 nm = {-mB[b], -mB[b]}, rr = {rB[b], rB[b]};
      f32x2 v[8], t[8];
#pragma unroll
      for (int e = 0; e < 8; ++e) {
        f32x2 a2 = {av[b][2 * e], av[b][2 * e + 1]};
        v[e] = __builtin_elementwise_fma(__builtin_elementwise_fma(nm, Sv[e], a2), rr, Bv[e]);
      }
#pragma unroll
      for (int e = 0; e < 8; ++e) t[e] = __builtin_elementwise_fma(v[e] * v[e], (f32x2){c1, c1}, (f32x2){c0, c0});
#pragma unroll
      for (int e = 0; e < 8; ++e) t[e] = v[e] * t[e];
      __builtin_amdgcn_sched_barrier(0);
#pragma unroll
      for (int e = 0; e < 8; ++e) t[e] = (f32x2){__builtin_amdgcn_exp2f(t[e].x), __builtin_amdgcn_exp2f(t[e].y)};
#pragma unroll
      for (int e = 0; e < 8; ++e) t[e] = t[e] + 1.f;
      __builtin_amdgcn_sched_barrier(0);
#pragma unroll
      for (int e = 0; e < 8; ++e) t[e] = (f32x2){__builtin_amdgcn_rcpf(t[e].x), __builtin_amdgcn_rcpf(t[e].y)};
      unsigned o[8];
#pragma unroll
      for (int e = 0; e < 8; ++e) { f32x2 g = v[e] * t[e]; o[e] = pack2(g.x, g.y); }
      const int ltile = (l0 + wl * 64 + (b ^ wf) * 32) >> 5;
      const int fstep = (f0 + fl) >> 4;
#pragma unroll
      for (int s = 0; s < 2; ++s) {
        u32x4 ov = {o[4 * s], o[4 * s + 1], o[4 * s + 2], o[4 * s + 3]};
        asm volatile("global_store_dwordx4 %0, %1, off sc0 sc1\n\ts_nop 1" ::"v"(Hf + (((size_t)ltile * (FF / 16) + fstep + s) * 64 + lane) * 8), "v"(ov) : "memory");
      }
    }
  }
  STAMP(2, 3);
  asm volatile("s_waitcnt vmcnt(0) lgkmcnt(0)\n\ts_barrier" ::: "memory");
  if (tid == 0) {
    unsigned* fl = flags + lt * 64;
    __hip_atomic_fetch_add(fl, 1u, __ATOMIC_RELAXED, __HIP_MEMORY_SCOPE_AGENT);
    unsigned spins = 0;
    while (__hip_atomic_load(fl, __ATOMIC_RELAXED, __HIP_MEMORY_SCOPE_AGENT) < 14u) {
      __builtin_amdgcn_s_sleep(1);
      if (++spins > (1u << 24)) break;
    }
  }
  asm volatile("s_waitcnt vmcnt(0) lgkmcnt(0)\n\ts_barrier" ::: "memory");
  STAMP(2, 4);
  {
    const unsigned short* hsrc2 = Hf + (((size_t)(lt2 * 2) * (FF / 16) + wv) * 64 + lane) * 8;
    auto stage2 = [&](int c) {
      char* base = smem + (c & 7) * 16384;
#pragma unroll
      for (int i = 0; i < 2; ++i)
        __builtin_amdgcn_global_load_lds((const unsigned*)(hsrc2 + ((size_t)i * (FF / 16) + c * 8) * 512),
                                         (unsigned*)(base + (i * 8 + wv) * 1024), 16, 0, 16);
    };
#pragma unroll
    for (int c = 0; c < 8; ++c) stage2(c);
    f32x16 ac2[2];
#pragma unroll
    for (int b = 0; b < 2; ++b)
#pragma unroll
      for (int e = 0; e < 16; ++e) ac2[b][e] = 0.f;
    const unsigned aoff = lb + kq * 1024 + lane * 16;
    u32x4 Hr[2][2][2];
    WAIT_V_BAR(14);
    STAMP(2, 5);
#pragma unroll
    for (int j = 0; j < 2; ++j)
#pragma unroll
      for (int b = 0; b < 2; ++b) DS_B128(Hr[0][j][b], aoff, j * 4096 + b * 8192);
#pragma unroll
    for (int c = 0; c < 14; ++c) {
      const int set = c & 1;
      if (c < 13) {
        if (c < 7) { WAIT_V_BAR(12); }
        else if (c == 7) { WAIT_V_BAR(10); } else if (c == 8) { WAIT_V_BAR(8); } else if (c == 9) { WAIT_V_BAR(6); }
        else if (c == 10) { WAIT_V_BAR(4); } else if (c == 11) { WAIT_V_BAR(2); } else { WAIT_V_BAR(0); }
        if (c + 8 < 14) stage2(c + 8);
      } else {
        WAIT_L0();
      }
      asm volatile("" : "+v"(Hr[set][0][0]), "+v"(Hr[set][0][1]), "+v"(Hr[set][1][0]), "+v"(Hr[set][1][1]));
      if (c == 0) {
#pragma unroll
        for (int i = 0; i < 28; i += 4) asm volatile("" : "+v"(Wf[i]), "+v"(Wf[i + 1]), "+v"(Wf[i + 2]), "+v"(Wf[i + 3]));
      }
      const unsigned aa = aoff + ((c + 1) & 7) * 16384;
      __builtin_amdgcn_sched_barrier(0);
#pragma unroll
      for (int j = 0; j < 2; ++j)
#pragma unroll
        for (int b = 0; b < 2; ++b) {
          ac2[b] = __builtin_amdgcn_mfma_f32_32x32x16_bf16(__builtin_bit_cast(bf16x8, Wf[2 * c + j]),
                                                           __builtin_bit_cast(bf16x8, Hr[set][j][b]), ac2[b], 0, 0, 0);
          if (c < 13) DS_B128(Hr[set ^ 1][j][b], aa, j * 4096 + b * 8192);
          __builtin_amdgcn_sched_barrier(0);
        }
    }
    STAMP(2, 6);
    RAW_BAR();
    {
      f32x4* xb = (f32x4*)(smem + wv * 8192) + lane;
#pragma unroll
      for (int Q = 0; Q < 4; ++Q)
#pragma unroll
        for (int g = 0; g < 2; ++g) {
          const int r = 8 * (Q & 1) + 4 * g;
          xb[(Q * 2 + g) * 64] = (f32x4){ac2[Q >> 1][r], ac2[Q >> 1][r + 1], ac2[Q >> 1][r + 2], ac2[Q >> 1][r + 3]};
        }
    }
    RAW_BAR();
#pragma unroll
    for (int g = 0; g < 2; ++g) {
      f32x4 t = {0.f, 0.f, 0.f, 0.f};
#pragma unroll
      for (int k2 = 0; k2 < 4; ++k2) t += *((const f32x4*)(smem + (k2 * 2 + wd2) * 8192) + (kq * 2 + g) * 64 + lane);
      float bv[4] = {bflo(bres[g][0]), bfhi(bres[g][0]), bflo(bres[g][1]), bfhi(bres[g][1])};
      float o[4];
#pragma unroll
      for (int e = 0; e < 4; ++e) o[e] = bf2f(hres[4 * g + e]) + (t[e] + bv[e]);
      u32x2 ov = {pack2(o[0], o[1]), pack2(o[2], o[3])};
      *(u32x2*)(out + (size_t)lrow * DD + dbase + 8 * g) = ov;
    }
  }
  STAMP(2, 7);
}

extern "C" void kernel_launch(void* const* d_in, const int* in_sizes, int n_in, void* d_out,
                              int out_size, void* d_ws, size_t ws_size, hipStream_t stream) {
  const unsigned short* x = (const unsigned short*)d_in[0];
  const unsigned short* logA = (const unsigned short*)d_in[1];
  const unsigned short* Aim = (const unsigned short*)d_in[2];
  const unsigned short* Cre = (const unsigned short*)d_in[3];
  const unsigned short* Cim = (const unsigned short*)d_in[4];
  const unsigned short* logdt = (const unsigned short*)d_in[5];
  const unsigned short* Dskip = (const unsigned short*)d_in[6];
  const unsigned short* w1 = (const unsigned short*)d_in[8];
  const unsigned short* b1 = (const unsigned short*)d_in[9];
  const unsigned short* w2 = (const unsigned short*)d_in[10];
  const unsigned short* b2 = (const unsigned short*)d_in[11];
  unsigned short* out = (unsigned short*)d_out;

  char* ws = (char*)d_ws;
  const size_t T = (size_t)DD * LL * 2;
  unsigned short* uT = (unsigned short*)(ws);
  unsigned short* xT = (unsigned short*)(ws + T);
  unsigned short* Kx = (unsigned short*)(ws + 2 * T);
  unsigned short* hT = (unsigned short*)(ws + 3 * T);
  float* S1 = (float*)(ws + 4 * T);
  unsigned short* Hf = (unsigned short*)(ws + 4 * T + 8192);
  u32x4* W2f = (u32x4*)(ws + 4 * T + 8192 + (size_t)LL * FF * 2);
  unsigned* flags = (unsigned*)(ws + 4 * T + 8192 + (size_t)LL * FF * 2 + (size_t)FF * DD * 2);

  (void)hipFuncSetAttribute((const void*)k34_kernel, hipFuncAttributeMaxDynamicSharedMemorySize, K3_LDS);

  k1_kernel<<<K1_KG_BLOCKS + K1_LN_BLOCKS, 256, 0, stream>>>(x, logA, Aim, Cre, Cim, logdt, w1, w2, uT, xT, Kx, flags);
  k2_kernel<<<DD + K2_AUX, 256, 0, stream>>>(uT, xT, Kx, Dskip, w1, w2, hT, S1, W2f);
  k34_kernel<<<224, 512, K3_LDS, stream>>>(hT, w1, S1, b1, W2f, b2, Hf, flags, out);
}
```

```cpp
#include <hip/hip_runtime.h>
#include <hip/hip_bf16.h>
#include <stdint.h>

#define LL 2048
#define DD 448
#define NN 256
#define FF 1792

typedef __bf16 bf16x2 __attribute__((ext_vector_type(2)));
typedef __bf16 bf16x8 __attribute__((ext_vector_type(8)));
typedef _Float16 f16x2 __attribute__((ext_vector_type(2)));
typedef _Float16 f16x8 __attribute__((ext_vector_type(8)));
typedef float f32x2 __attribute__((ext_vector_type(2)));
typedef float f32x4 __attribute__((ext_vector_type(4)));
typedef float f32x16 __attribute__((ext_vector_type(16)));
typedef unsigned u32x4 __attribute__((ext_vector_type(4)));
typedef unsigned u32x2 __attribute__((ext_vector_type(2)));

#define DI __device__ __forceinline__

#define STAMP(k, i)

DI float bfr(float x) { return (float)(__bf16)x; }
DI float bf2f(unsigned short v) { return __uint_as_float(((unsigned)v) << 16); }
DI float bflo(unsigned w) { return __uint_as_float(w << 16); }
DI float bfhi(unsigned w) { return __uint_as_float(w & 0xffff0000u); }
DI unsigned pack2(float a, float b) {
  bf16x2 r = {(__bf16)a, (__bf16)b};
  return __builtin_bit_cast(unsigned, r);
}
DI unsigned packh(f32x2 a) {
  f16x2 r = {(_Float16)a.x, (_Float16)a.y};
  return __builtin_bit_cast(unsigned, r);
}
DI unsigned lds_addr(const void* p) {
  return (unsigned)(uintptr_t)(__attribute__((address_space(3))) const char*)p;
}

#define WAIT_V_BAR(n) asm volatile("s_waitcnt vmcnt(" #n ") lgkmcnt(0)\n\ts_barrier" ::: "memory")
#define WAIT_L0() asm volatile("s_waitcnt lgkmcnt(0)" ::: "memory")
#define DS_TR(dst, addr, off) asm volatile("ds_read_b64_tr_b16 %0, %1 offset:%2" : "=v"(dst) : "v"(addr), "i"(off))
#define DS_B128(dst, addr, off) asm volatile("ds_read_b128 %0, %1 offset:%2" : "=v"(dst) : "v"(addr), "i"(off))

DI void st_wt16(void* p, u32x4 v) { asm volatile("global_store_dwordx4 %0, %1, off sc0 sc1\n\ts_nop 1" ::"v"(p), "v"(v) : "memory"); }
DI void st_wt8(void* p, u32x2 v) { asm volatile("global_store_dwordx2 %0, %1, off sc0 sc1\n\ts_nop 1" ::"v"(p), "v"(v) : "memory"); }

DI bf16x8 cat_frag(u32x2 lo, u32x2 hi) {
  u32x4 v = __builtin_shufflevector(lo, hi, 0, 1, 2, 3);
  return __builtin_bit_cast(bf16x8, v);
}

#define K1_LN_BLOCKS 64
#define K1_KG_BLOCKS DD
#define K1_ROWB 272
#define K1_WREG (48 * K1_ROWB)
#define K1_LNROW 928

DI f32x2 cexp_p(float lr2, float lih, float lil, float p) {
  float mag = __builtin_amdgcn_exp2f(lr2 * p);
  float ph = lih * p;
  float e = __builtin_fmaf(lih, p, -ph);
  float fr = __builtin_amdgcn_fractf(ph) + __builtin_fmaf(lil, p, e);
  f32x2 cs = {__builtin_amdgcn_cosf(fr), __builtin_amdgcn_sinf(fr)};
  return cs * mag;
}
DI f32x2 crot(f32x2 w) { return (f32x2){-w.y, w.x}; }
DI f32x2 cmul(f32x2 a, f32x2 w, f32x2 wr) { return __builtin_elementwise_fma(a.yy, wr, a.xx * w); }
DI int cidx_of_lag(int lag) {
  __bf16 b = (__bf16)(float)lag;
  int bits = (int)__builtin_bit_cast(unsigned short, b);
  return lag < 128 ? lag : bits - (0x4300 - 128);
}
DI float sum8(float v) {
  v += __builtin_bit_cast(float, __builtin_amdgcn_mov_dpp(__builtin_bit_cast(int, v), 0xB1, 0xF, 0xF, true));
  v += __builtin_bit_cast(float, __builtin_amdgcn_mov_dpp(__builtin_bit_cast(int, v), 0x4E, 0xF, 0xF, true));
  v += __builtin_bit_cast(float, __builtin_amdgcn_mov_dpp(__builtin_bit_cast(int, v), 0x141, 0xF, 0xF, true));
  return v;
}
typedef short s16x4 __attribute__((ext_vector_type(4)));
typedef __attribute__((address_space(3))) s16x4* lds_s16x4_ptr;

__global__ __launch_bounds__(256) void k1_kernel(
    const unsigned short* __restrict__ x, const unsigned short* __restrict__ logA,
    const unsigned short* __restrict__ Aim, const unsigned short* __restrict__ Cre,
    const unsigned short* __restrict__ Cim, const unsigned short* __restrict__ logdt,
    const unsigned short* __restrict__ w1, const unsigned short* __restrict__ w2,
    unsigned short* __restrict__ uT, unsigned short* __restrict__ xT,
    unsigned short* __restrict__ Kx, unsigned* __restrict__ flags) {
  __shared__ __attribute__((aligned(16))) char smem[4 * K1_WREG];
  const int tid = threadIdx.x, lane = tid & 63, wv = __builtin_amdgcn_readfirstlane(tid >> 6);
  const int bid = blockIdx.x;
  STAMP(0, 0);
  if (bid == 0 && tid < 16) flags[tid * 64] = 0u;
  if (bid >= K1_KG_BLOCKS) {
    __builtin_amdgcn_s_setprio(3);
    const int l0 = (bid - K1_KG_BLOCKS) * 32;
    const int r = lane >> 3, c = lane & 7;
    const int row = wv * 8 + r;
    u32x4 raw[7];
#pragma unroll
    for (int i = 0; i < 7; ++i) raw[i] = *(const u32x4*)(x + (size_t)(l0 + row) * DD + (c + 8 * i) * 8);
    f32x2 s2 = {0.f, 0.f};
#pragma unroll
    for (int i = 0; i < 7; ++i)
#pragma unroll
      for (int e = 0; e < 4; ++e) s2 += (f32x2){bflo(raw[i][e]), bfhi(raw[i][e])};
    const float mean = sum8(s2.x + s2.y) * (1.f / DD);
    f32x2 q2 = {0.f, 0.f};
    const f32x2 mm = {mean, mean};
#pragma unroll
    for (int i = 0; i < 7; ++i)
#pragma unroll
      for (int e = 0; e < 4; ++e) { f32x2 t = (f32x2){bflo(raw[i][e]), bfhi(raw[i][e])} - mm; q2 = __builtin_elementwise_fma(t, t, q2); }
    const float var = sum8(q2.x + q2.y) * (1.f / DD);
    const float m = bfr(mean);
    const float sd = bfr(sqrtf(bfr(bfr(var) + 1e-5f)));
    const float rs = 1.f / sd;
    const f32x2 m2 = {m, m}, rs2 = {rs, rs}, nsd2 = {-sd, -sd};
    char* tu = smem;
#pragma unroll
    for (int i = 0; i < 7; ++i) {
      u32x4 o;
#pragma unroll
      for (int e = 0; e < 4; ++e) {
        f32x2 t = (f32x2){bflo(raw[i][e]), bfhi(raw[i][e])} - m2;
        unsigned tb = pack2(t.x, t.y);
        f32x2 dd = {bflo(tb), bfhi(tb)};
        f32x2 qv = dd * rs2;
        f32x2 er = __builtin_elementwise_fma(qv, nsd2, dd);
        qv = __builtin_elementwise_fma(er, rs2, qv);
        o[e] = pack2(qv.x, qv.y);
      }
      *(u32x4*)(tu + row * K1_LNROW + (c + 8 * i) * 16) = o;
    }
    __syncthreads();
    const int g = lane >> 4, q = (lane & 15) >> 2, pp = lane & 3;
    const char* rb = tu + (8 * g + q) * K1_LNROW + pp * 8;
#pragma unroll
    for (int it = 0; it < 7; ++it) {
      const int c0 = (it * 4 + wv) * 16;
      s16x4 a = __builtin_amdgcn_ds_read_tr16_b64_v4i16((lds_s16x4_ptr)(rb + c0 * 2));
      s16x4 b = __builtin_amdgcn_ds_read_tr16_b64_v4i16((lds_s16x4_ptr)(rb + 4 * K1_LNROW + c0 * 2));
      u32x2 a2 = __builtin_bit_cast(u32x2, a), b2 = __builtin_bit_cast(u32x2, b);
      u32x4 o = {a2[0], a2[1], b2[0], b2[1]};
      st_wt16(uT + (size_t)(c0 + (lane & 15)) * LL + l0 + 8 * g, o);
    }
    __syncthreads();
#pragma unroll
    for (int i = 0; i < 7; ++i) *(u32x4*)(tu + row * K1_LNROW + (c + 8 * i) * 16) = raw[i];
    __syncthreads();
#pragma unroll
    for (int it = 0; it < 7; ++it) {
      const int c0 = (it * 4 + wv) * 16;
      s16x4 a = __builtin_amdgcn_ds_read_tr16_b64_v4i16((lds_s16x4_ptr)(rb + c0 * 2));
      s16x4 b = __builtin_amdgcn_ds_read_tr16_b64_v4i16((lds_s16x4_ptr)(rb + 4 * K1_LNROW + c0 * 2));
      u32x2 a2 = __builtin_bit_cast(u32x2, a), b2 = __builtin_bit_cast(u32x2, b);
      u32x4 o = {a2[0], a2[1], b2[0], b2[1]};
      st_wt16(xT + (size_t)(c0 + (lane & 15)) * LL + l0 + 8 * g, o);
    }
  } else {
    const int d = bid;
    const int nb = d * NN + tid;
    float lr2, lih, lil, cr, ci;
    u32x4 pf1, pf2;
    {
      const unsigned short la = logA[nb], ai = Aim[nb], cre = Cre[nb], cim = Cim[nb];
      const unsigned short ldt = logdt[d];
      const float dt = bfr(expf(bf2f(ldt)));
      const float I2PH = 0.15915494f;
      const float I2PL = 6.4206382e-09f;
      const float lr = -bfr(expf(bf2f(la))) * dt;
      const float li = bf2f(ai) * dt;
      lr2 = lr * 1.44269504f;
      lih = li * I2PH;
      lil = __builtin_fmaf(li, I2PH, -lih) + li * I2PL;
      cr = 2.f * bf2f(cre) * dt;
      ci = 2.f * bf2f(cim) * dt;
    }
    float cm = fmaxf(fabsf(cr), fabsf(ci));
#pragma unroll
    for (int o = 32; o > 0; o >>= 1) cm = fmaxf(cm, __shfl_xor(cm, o));
    const float scale = cm > 0.f ? cm : 1.f;
    const float inv = 1.f / scale;
    STAMP(0, 1);
    {
      const int pi = d * 224 + (tid < 224 ? tid : 223);
      asm volatile("global_load_dwordx4 %0, %1, off" : "=v"(pf1) : "v"((const u32x4*)w1 + pi) : "memory");
      asm volatile("global_load_dwordx4 %0, %1, off" : "=v"(pf2) : "v"((const u32x4*)w2 + pi) : "memory");
    }
    char* reg = smem + wv * K1_WREG;
    unsigned* wr_ = (unsigned*)(reg + lane * 4);
#define KROW(r) wr_[(r) * (K1_ROWB / 4)]
    const int i16 = lane & 15, kg = lane >> 4;
    const char* rdA = reg + i16 * K1_ROWB + kg * 16;
    f32x4 acc0 = {0, 0, 0, 0}, acc1 = {0, 0, 0, 0}, acc2 = {0, 0, 0, 0};
    {
      const f32x2 E1 = cexp_p(lr2, lih, lil, 1.f), E8 = cexp_p(lr2, lih, lil, 8.f), E128 = cexp_p(lr2, lih, lil, 128.f);
      const f32x2 E256 = cexp_p(lr2, lih, lil, 256.f), E512 = cexp_p(lr2, lih, lil, 512.f), E1024 = cexp_p(lr2, lih, lil, 1024.f);
      const f32x2 E1r = crot(E1), E128r = crot(E128), E256r = crot(E256), E512r = crot(E512);
      f32x2 a[16];
      {
        const f32x2 w = {E8.x, -E8.y}, wr = crot(w);
        const f32x2 w2 = cmul(w, w, wr), w2r = crot(w2);
        const f32x2 w4 = cmul(w2, w2, w2r), w4r = crot(w4);
        a[0] = (f32x2){cr * inv, -ci * inv};
        a[1] = cmul(a[0], w, wr);
        a[2] = cmul(a[0], w2, w2r);
        a[3] = cmul(a[1], w2, w2r);
#pragma unroll
        for (int i = 4; i < 16; ++i) a[i] = cmul(a[i - 4], w4, w4r);
      }
      f32x2 e[8];
      e[0] = (f32x2){1.f, 0.f}; e[1] = E1;
      e[2] = cmul(E1, E1, E1r);
      e[3] = cmul(e[2], E1, E1r);
      const f32x2 e2r = crot(e[2]);
      e[4] = cmul(e[2], e[2], e2r);
      const f32x2 e4r = crot(e[4]);
      e[5] = cmul(e[1], e[4], e4r); e[6] = cmul(e[2], e[4], e4r); e[7] = cmul(e[3], e[4], e4r);
#pragma unroll
      for (int i = 0; i < 16; ++i) KROW(i) = packh(a[i]);
#pragma unroll
      for (int j = 0; j < 8; ++j) { KROW(16 + j) = packh(e[j]); KROW(24 + j) = packh(j ? cmul(e[j], E128, E128r) : E128); }
      const f32x2 E384 = cmul(E256, E128, E128r), E384r = crot(E384);
#pragma unroll
      for (int r = 0; r < 4; ++r) {
        KROW(32 + r) = packh(r ? cmul(e[2 * r], E256, E256r) : E256);
        KROW(36 + r) = packh(r ? cmul(e[2 * r], E384, E384r) : E384);
      }
      f32x2 u[4];
      u[0] = E512; u[1] = cmul(E512, E128, E128r); u[2] = cmul(E512, E256, E256r); u[3] = cmul(E512, E384, E384r);
#pragma unroll
      for (int w = 0; w < 4; ++w) { KROW(40 + 2 * w) = packh(u[w]); KROW(41 + 2 * w) = packh(cmul(u[w], e[4], e4r)); }
      f32x2 v[9];
      v[0] = E1024; v[1] = cmul(E1024, E128, E128r); v[2] = cmul(E1024, E256, E256r); v[3] = cmul(E1024, E384, E384r);
#pragma unroll
      for (int c = 4; c < 9; ++c) v[c] = cmul(v[c - 4], E512, E512r);
      unsigned vp[9];
#pragma unroll
      for (int c = 0; c < 9; ++c) vp[c] = packh(v[c]);
      __builtin_amdgcn_fence(__ATOMIC_RELEASE, "wavefront");
      __builtin_amdgcn_wave_barrier();
      __builtin_amdgcn_fence(__ATOMIC_ACQUIRE, "wavefront");
      f16x8 af[4];
#pragma unroll
      for (int s = 0; s < 4; ++s) {
        af[s] = *(const f16x8*)(rdA + 64 * s);
        f16x8 b0 = *(const f16x8*)(rdA + 16 * K1_ROWB + 64 * s);
        f16x8 b1 = *(const f16x8*)(rdA + 32 * K1_ROWB + 64 * s);
        acc0 = __builtin_amdgcn_mfma_f32_16x16x32_f16(af[s], b0, acc0, 0, 0, 0);
        acc1 = __builtin_amdgcn_mfma_f32_16x16x32_f16(af[s], b1, acc1, 0, 0, 0);
      }
      __builtin_amdgcn_fence(__ATOMIC_RELEASE, "wavefront");
      __builtin_amdgcn_wave_barrier();
      __builtin_amdgcn_fence(__ATOMIC_ACQUIRE, "wavefront");
#pragma unroll
      for (int c = 0; c < 9; ++c) KROW(16 + c) = vp[c];
      __builtin_amdgcn_fence(__ATOMIC_RELEASE, "wavefront");
      __builtin_amdgcn_wave_barrier();
      __builtin_amdgcn_fence(__ATOMIC_ACQUIRE, "wavefront");
#pragma unroll
      for (int s = 0; s < 4; ++s) {
        f16x8 b2 = *(const f16x8*)(rdA + 16 * K1_ROWB + 64 * s);
        acc2 = __builtin_amdgcn_mfma_f32_16x16x32_f16(af[s], b2, acc2, 0, 0, 0);
      }
    }
#undef KROW
    acc0 *= scale; acc1 *= scale; acc2 *= scale;
    __syncthreads();
    STAMP(0, 2);
    float* red = (float*)smem;
    unsigned short* kd = (unsigned short*)(smem + 12288);
    *(f32x4*)(red + ((wv * 3 + 0) * 64 + lane) * 4) = acc0;
    *(f32x4*)(red + ((wv * 3 + 1) * 64 + lane) * 4) = acc1;
    *(f32x4*)(red + ((wv * 3 + 2) * 64 + lane) * 4) = acc2;
    __syncthreads();
    if (wv < 3) {
      f32x4 t = *(const f32x4*)(red + ((0 * 3 + wv) * 64 + lane) * 4);
#pragma unroll
      for (int w = 1; w < 4; ++w) t += *(const f32x4*)(red + ((w * 3 + wv) * 64 + lane) * 4);
      const int j = i16;
      const int lo0 = 128 * (j >> 3) + (j & 7);
      const int lo1 = j < 8 ? 256 + 128 * (j >> 2) + 2 * (j & 3) : 512 + 128 * ((j - 8) >> 1) + 4 * ((j - 8) & 1);
      const int lo2 = 1024 + 128 * j;
      const int lo = wv == 0 ? lo0 : (wv == 1 ? lo1 : lo2);
#pragma unroll
      for (int r = 0; r < 4; ++r) {
        const int pos = 8 * (4 * kg + r) + lo;
        __bf16 v0 = (__bf16)t[r];
        if (pos <= 2048) kd[cidx_of_lag(pos)] = __builtin_bit_cast(unsigned short, v0);
      }
    }
    __syncthreads();
    {
      unsigned o[4];
#pragma unroll
      for (int e = 0; e < 4; ++e) {
        const int lag = 2047 - (8 * tid + 2 * e);
        unsigned a = kd[cidx_of_lag(lag)], b = kd[cidx_of_lag(lag - 1)];
        o[e] = a | (b << 16);
      }
      u32x4 o0 = {o[0], o[1], o[2], o[3]};
      st_wt16(Kx + (size_t)d * LL + 8 * tid, o0);
    }
    asm volatile("" ::"v"(pf1), "v"(pf2));
  }
  STAMP(0, 3);
}

#define K2_CSTRIDE 4160
#define K2_CS_BLOCKS 28
#define K2_PK_BLOCKS 56
#define K2_AUX (K2_CS_BLOCKS + K2_PK_BLOCKS)
__global__ __launch_bounds__(256) void k2_kernel(
    const unsigned short* __restrict__ uT, const unsigned short* __restrict__ xT,
    const unsigned short* __restrict__ Kx, const unsigned short* __restrict__ Dskip,
    const unsigned short* __restrict__ w1, const unsigned short* __restrict__ w2,
    unsigned short* __restrict__ hT, float* __restrict__ S1, u32x4* __restrict__ W2f) {
  __shared__ __attribute__((aligned(16))) char smem[8 * K2_CSTRIDE + 4608];
  char* Kc = smem;
  char* Ul = smem + 8 * K2_CSTRIDE;
  const int tid = threadIdx.x, lane = tid & 63, wv = __builtin_amdgcn_readfirstlane(tid >> 6);
  STAMP(1, 0);
  const int abid = (int)blockIdx.x - DD;
  if (abid >= K2_PK_BLOCKS) {
    const int f0 = (abid - K2_PK_BLOCKS) * 64;
    float* red = (float*)smem;
    const int fc = tid & 7, rg = tid >> 3;
    u32x4 raw[14];
#pragma unroll
    for (int r = 0; r < 14; ++r) raw[r] = *(const u32x4*)(w1 + (size_t)(rg * 14 + r) * FF + f0 + fc * 8);
    f32x2 s[4] = {{0.f, 0.f}, {0.f, 0.f}, {0.f, 0.f}, {0.f, 0.f}};
#pragma unroll
    for (int r = 0; r < 14; ++r)
#pragma unroll
      for (int e = 0; e < 4; ++e) s[e] += (f32x2){bflo(raw[r][e]), bfhi(raw[r][e])};
    *(f32x4*)(red + rg * 64 + fc * 8) = (f32x4){s[0].x, s[0].y, s[1].x, s[1].y};
    *(f32x4*)(red + rg * 64 + fc * 8 + 4) = (f32x4){s[2].x, s[2].y, s[3].x, s[3].y};
    __syncthreads();
    if (tid < 64) {
      float t = 0.f;
#pragma unroll
      for (int r = 0; r < 32; ++r) t += red[r * 64 + tid];
      S1[f0 + tid] = t;
    }
    STAMP(1, 3);
    return;
  }
  if (abid >= 0) {
    const int f0 = abid * 32;
    u32x4 raw[7];
#pragma unroll
    for (int i = 0; i < 7; ++i) { const int e = tid + 256 * i; raw[i] = *(const u32x4*)(w2 + (size_t)(f0 + e / 56) * DD + (e % 56) * 8); }
#pragma unroll
    for (int i = 0; i < 7; ++i) { const int e = tid + 256 * i; *(u32x4*)(smem + (e / 56) * K1_LNROW + (e % 56) * 16) = raw[i]; }
    __syncthreads();
    const int hh = lane >> 5, blk = (lane >> 4) & 1, q = (lane & 15) >> 2, pp = lane & 3;
#pragma unroll
    for (int i = 0; i < 7; ++i) {
      const int fr = i * 4 + wv;
      const int dg = fr >> 1, sl = fr & 1;
      const char* rb = smem + (16 * sl + 4 * hh + q) * K1_LNROW + (dg * 32 + 16 * blk + 4 * pp) * 2;
      s16x4 a = __builtin_amdgcn_ds_read_tr16_b64_v4i16((lds_s16x4_ptr)rb);
      s16x4 b = __builtin_amdgcn_ds_read_tr16_b64_v4i16((lds_s16x4_ptr)(rb + 8 * K1_LNROW));
      u32x2 a2 = __builtin_bit_cast(u32x2, a), b2 = __builtin_bit_cast(u32x2, b);
      const int st = (f0 >> 4) + sl;
      st_wt16(W2f + ((size_t)(dg * 4 + (st & 3)) * 28 + (st >> 2)) * 64 + lane, (u32x4){a2[0], a2[1], b2[0], b2[1]});
    }
    STAMP(1, 3);
    return;
  }
  const int d = blockIdx.x;
  const int b = lane & 15, h = lane >> 4;
  u32x2 xx[2];
  float Dk;
  {
    const u32x4* kp = (const u32x4*)(Kx + (size_t)d * LL);
    u32x4 w0 = kp[tid];
    u32x4 w1 = {0, 0, 0, 0};
    if (tid < 255) w1 = kp[tid + 1];
    u32x4 uu = *(const u32x4*)(uT + (size_t)d * LL + tid * 8);
#pragma unroll
    for (int tt = 0; tt < 2; ++tt) xx[tt] = *(const u32x2*)(xT + (size_t)d * LL + 256 * (2 * wv + tt) + 16 * b + 4 * h);
    Dk = bf2f(Dskip[d]);
    unsigned w[8] = {w0[0], w0[1], w0[2], w0[3], w1[0], w1[1], w1[2], w1[3]};
#pragma unroll
    for (int m = 0; m < 4; ++m) {
      u32x4 ev = {w[m], w[m + 1], w[m + 2], w[m + 3]};
      u32x4 od;
#pragma unroll
      for (int i = 0; i < 4; ++i) od[i] = __builtin_amdgcn_alignbit(w[m + i + 1], w[m + i], 16);
      *(u32x4*)(Kc + (2 * m) * K2_CSTRIDE + tid * 16) = ev;
      *(u32x4*)(Kc + (2 * m + 1) * K2_CSTRIDE + tid * 16) = od;
    }
    u32x4 z = {0, 0, 0, 0};
    if (tid < 8) *(u32x4*)(Kc + tid * K2_CSTRIDE + 4096) = z;
    if (tid < 32) *(u32x4*)(Ul + tid * 16) = z;
    *(u32x4*)(Ul + 512 + tid * 16) = uu;
  }
  __syncthreads();
  STAMP(1, 1);
  f32x4 acc[8];
#pragma unroll
  for (int t = 0; t < 8; ++t) acc[t] = (f32x4){0, 0, 0, 0};
  const char* fbase = Kc + (7 - (b & 7)) * K2_CSTRIDE + 2 * (2040 - 8 * (b >> 3) + 8 * h);
  const char* gbase = Ul + 512 + 2 * (16 * b + 8 * h);
#pragma unroll
  for (int rr = 0; rr < 2; ++rr) {
    const int r = 4 * wv + 1 + 2 * rr;
    bf16x8 Fq[8], Gs[8];
#pragma unroll
    for (int q = 0; q < 8; ++q) Fq[q] = *(const bf16x8*)(fbase - 32 * (16 * q + r));
#pragma unroll
    for (int s = 0; s < 8; ++s) Gs[s] = *(const bf16x8*)(gbase + 32 * (16 * s - r));
#pragma unroll
    for (int q = 0; q < 8; ++q)
#pragma unroll
      for (int s = 0; s < 8 - q; ++s)
        acc[q + s] = __builtin_amdgcn_mfma_f32_16x16x32_bf16(Fq[q], Gs[s], acc[q + s], 0, 0, 0);
  }
  __syncthreads();
  STAMP(1, 2);
  float* red = (float*)smem;
#pragma unroll
  for (int t = 0; t < 8; ++t) *(f32x4*)(red + ((wv * 8 + t) * 64 + lane) * 4) = acc[t];
  __syncthreads();
#pragma unroll
  for (int tt = 0; tt < 2; ++tt) {
    const int t = 2 * wv + tt;
    f32x4 y = *(const f32x4*)(red + ((0 * 8 + t) * 64 + lane) * 4);
#pragma unroll
    for (int w = 1; w < 4; ++w) y += *(const f32x4*)(red + ((w * 8 + t) * 64 + lane) * 4);
    const int l = 256 * t + 16 * b + 4 * h;
    u32x2 uu = *(const u32x2*)(Ul + 512 + 2 * l);
    float u0 = bflo(uu[0]), u1 = bfhi(uu[0]), u2 = bflo(uu[1]), u3 = bfhi(uu[1]);
    float x0 = bflo(xx[tt][0]), x1 = bfhi(xx[tt][0]), x2 = bflo(xx[tt][1]), x3 = bfhi(xx[tt][1]);
    float h0 = x0 + bfr(bfr(y[0]) + bfr(u0 * Dk));
    float h1 = x1 + bfr(bfr(y[1]) + bfr(u1 * Dk));
    float h2 = x2 + bfr(bfr(y[2]) + bfr(u2 * Dk));
    float h3 = x3 + bfr(bfr(y[3]) + bfr(u3 * Dk));
    u32x2 o = {pack2(h0, h1), pack2(h2, h3)};
    st_wt8(hT + (size_t)d * LL + l, o);
  }
  STAMP(1, 3);
}

#define K3_NBUF 4
#define K3_CHUNK 32768
#define K3_XOFF (K3_NBUF * K3_CHUNK)
#define K3_LDS (K3_XOFF + 4096)
#define RAW_BAR() asm volatile("s_waitcnt lgkmcnt(0)\n\ts_barrier" ::: "memory")
__global__ __launch_bounds__(512) void k34_kernel(
    const unsigned short* __restrict__ hT, const unsigned short* __restrict__ w1,
    const float* __restrict__ S1, const unsigned short* __restrict__ b1,
    const u32x4* __restrict__ W2f, const unsigned short* __restrict__ b2,
    unsigned short* Hf, unsigned* flags, unsigned short* __restrict__ out) {
  extern __shared__ __attribute__((aligned(16))) char smem[];
  const int tid = threadIdx.x, lane = tid & 63, wv = __builtin_amdgcn_readfirstlane(tid >> 6);
  const int bid = blockIdx.x;
  const int xcd = bid & 7, idx = bid >> 3;
  const int lt = 2 * xcd + (idx / 14), ft = idx % 14;
  const int l0 = lt * 128, f0 = ft * 128;
  const int wf = wv >> 2, wl = wv & 3;

  const int srow = lane >> 4, sch = lane & 15;
  const unsigned short* wsrc = w1 + (size_t)(wv * 4 + srow) * FF + f0 + (sch ^ ((srow & 3) << 2)) * 8;
  const unsigned short* hsrc = hT + (size_t)(wv * 4 + srow) * LL + l0 + (sch ^ ((srow & 3) << 2)) * 8;
  auto stage = [&](int c, int buf) {
    char* base = smem + buf * K3_CHUNK;
#pragma unroll
    for (int i = 0; i < 2; ++i) {
      const int k = c * 64 + i * 32;
      __builtin_amdgcn_global_load_lds((const unsigned*)(wsrc + (size_t)k * FF),
                                       (unsigned*)(base + (i * 8 + wv) * 1024), 16, 0, 0);
      __builtin_amdgcn_global_load_lds((const unsigned*)(hsrc + (size_t)k * LL),
                                       (unsigned*)(base + 16384 + (i * 8 + wv) * 1024), 16, 0, 0);
    }
  };
  STAMP(2, 0);
  const int lt2 = 4 * xcd + (idx / 7), dt2 = idx % 7;
  const int kq = wv >> 1, wd2 = wv & 1;
  const int lrow = lt2 * 64 + (kq >> 1) * 32 + (lane & 31);
  const int dbase = dt2 * 64 + wd2 * 32 + 4 * (lane >> 5) + 16 * (kq & 1);
  if (wv < 2) __builtin_amdgcn_global_load_lds((const unsigned*)(S1 + f0 + tid), (unsigned*)(smem + K3_XOFF + wv * 256), 4, 0, 0);
  else if (wv == 2) __builtin_amdgcn_global_load_lds((const unsigned*)(b1 + f0 + lane * 2), (unsigned*)(smem + K3_XOFF + 512), 4, 0, 0);
  stage(0, 0); stage(1, 1); stage(2, 2); stage(3, 3);

  f32x16 acc[2];
#pragma unroll
  for (int a = 0; a < 2; ++a)
#pragma unroll
    for (int e = 0; e < 16; ++e) acc[a][e] = 0.f;
  float sm = 0.f, sq = 0.f;

  const int hh = lane >> 5, blk = (lane >> 4) & 1, q = (lane & 15) >> 2, p = lane & 3;
  const unsigned lb = lds_addr(smem);
  unsigned roff[3];
#pragma unroll
  for (int c = 0; c < 2; ++c)
    roff[c] = lb + (8 * hh + q) * 256 + (4 * ((2 * wf + c) ^ q) + 2 * blk + (p >> 1)) * 16 + 8 * (p & 1);
  roff[2] = lb + 16384 + (8 * hh + q) * 256 + (4 * (wl ^ q) + 2 * blk + (p >> 1)) * 16 + 8 * (p & 1);
  const bf16x2 ones = {(__bf16)1.0f, (__bf16)1.0f};
  u32x2 Rr[2][2][3][2];
  u32x4 Wf[28];
  const u32x4* wp = W2f + ((size_t)((dt2 * 2 + wd2) * 4 + kq) * 28) * 64 + lane;
#define WLOAD(i) asm volatile("global_load_dwordx4 %0, %1, off offset:%2" : "=v"(Wf[i]) : "v"(wp + ((i) & ~3) * 64), "i"(((i) & 3) * 1024) : "memory")
  WAIT_V_BAR(12);
  STAMP(2, 1);
#pragma unroll
  for (int ks = 0; ks < 2; ++ks)
#pragma unroll
    for (int o = 0; o < 3; ++o) { DS_TR(Rr[0][ks][o][0], roff[o], ks * 4096); DS_TR(Rr[0][ks][o][1], roff[o], ks * 4096 + 1024); }
#pragma unroll
  for (int h = 0; h < 14; ++h) {
    const int set = h & 1, c = h >> 1;
    if (h & 1) {
      if (c == 0) { WAIT_V_BAR(8); } else if (c == 1) { WAIT_V_BAR(10); } else if (c == 2) { WAIT_V_BAR(12); }
      else if (c == 3) { WAIT_V_BAR(14); } else if (c == 4) { WAIT_V_BAR(10); } else if (c == 5) { WAIT_V_BAR(6); }
      else { WAIT_L0(); }
      if (c + 4 < 7) stage(c + 4, c & 3);
      WLOAD(2 * c); WLOAD(2 * c + 1);
    } else {
      WAIT_L0();
    }
#pragma unroll
    for (int ks = 0; ks < 2; ++ks)
      asm volatile("" : "+v"(Rr[set][ks][0][0]), "+v"(Rr[set][ks][0][1]), "+v"(Rr[set][ks][1][0]), "+v"(Rr[set][ks][1][1]),
                        "+v"(Rr[set][ks][2][0]), "+v"(Rr[set][ks][2][1]));
    unsigned ra[3];
#pragma unroll
    for (int o = 0; o < 3; ++o) ra[o] = roff[o] + (((h + 1) >> 1) & 3) * K3_CHUNK + ((h + 1) & 1) * 8192;
    __builtin_amdgcn_sched_barrier(0);
#pragma unroll
    for (int ks = 0; ks < 2; ++ks) {
      bf16x8 fr[3];
#pragma unroll
      for (int o = 0; o < 3; ++o) fr[o] = cat_frag(Rr[set][ks][o][0], Rr[set][ks][o][1]);
#pragma unroll
      for (int a = 0; a < 2; ++a) {
        acc[a] = __builtin_amdgcn_mfma_f32_32x32x16_bf16(fr[a], fr[2], acc[a], 0, 0, 0);
        if (h < 13) {
          DS_TR(Rr[set ^ 1][ks][a][0], ra[a], ks * 4096); DS_TR(Rr[set ^ 1][ks][a][1], ra[a], ks * 4096 + 1024);
          DS_TR(Rr[set ^ 1][ks][2][a], ra[2], ks * 4096 + a * 1024);
        }
        __builtin_amdgcn_sched_barrier(0);
      }
      if (ks == wf) {
#pragma unroll
        for (int e = 0; e < 4; ++e) {
          bf16x2 pr = {fr[2][2 * e], fr[2][2 * e + 1]};
          sm = __builtin_amdgcn_fdot2_f32_bf16(pr, ones, sm, false);
          sq = __builtin_amdgcn_fdot2_f32_bf16(pr, pr, sq, false);
        }
      }
      __builtin_amdgcn_sched_barrier(0);
    }
  }
  STAMP(2, 2);
  float mB, rB;
  {
    sm += __shfl_xor(sm, 32);
    sq += __shfl_xor(sq, 32);
    f32x2* st = (f32x2*)(smem + K3_XOFF + 1024);
    if (lane < 32) st[(wf * 4 + wl) * 32 + lane] = (f32x2){sm, sq};
    RAW_BAR();
    f32x2 o = st[((wf ^ 1) * 4 + wl) * 32 + (lane & 31)];
    const float mean = (sm + o.x) * (1.f / DD);
    const float var = fmaxf((sq + o.y) * (1.f / DD) - mean * mean, 0.f);
    mB = bfr(mean);
    rB = 1.f / bfr(sqrtf(bfr(bfr(var) + 1e-5f)));
  }
  const float* Ss = (const float*)(smem + K3_XOFF);
  const unsigned short* Bs = (const unsigned short*)(smem + K3_XOFF + 512);
  {
    const float c0 = -2.f * 0.7978845608028654f * 1.4426950408889634f;
    const float c1 = c0 * 0.044715f;
    const f32x2 nm = {-mB, -mB}, rr = {rB, rB};
    const int ltile = (l0 >> 5) + wl;
#pragma unroll
    for (int a = 0; a < 2; ++a) {
      const int fl = wf * 64 + a * 32;
      f32x2 v[8], t[8];
#pragma unroll
      for (int g = 0; g < 4; ++g) {
        f32x4 s4 = *(const f32x4*)(Ss + fl + 8 * g + 4 * hh);
        u32x2 b2v = *(const u32x2*)(Bs + fl + 8 * g + 4 * hh);
        f32x2 a0 = {acc[a][4 * g], acc[a][4 * g + 1]}, a1 = {acc[a][4 * g + 2], acc[a][4 * g + 3]};
        v[2 * g] = __builtin_elementwise_fma(__builtin_elementwise_fma(nm, (f32x2){s4[0], s4[1]}, a0), rr, (f32x2){bflo(b2v[0]), bfhi(b2v[0])});
        v[2 * g + 1] = __builtin_elementwise_fma(__builtin_elementwise_fma(nm, (f32x2){s4[2], s4[3]}, a1), rr, (f32x2){bflo(b2v[1]), bfhi(b2v[1])});
      }
#pragma unroll
      for (int e = 0; e < 8; ++e) t[e] = __builtin_elementwise_fma(v[e] * v[e], (f32x2){c1, c1}, (f32x2){c0, c0});
#pragma unroll
      for (int e = 0; e < 8; ++e) t[e] = v[e] * t[e];
      __builtin_amdgcn_sched_barrier(0);
      WLOAD(14 + 3 * a);
#pragma unroll
      for (int e = 0; e < 8; ++e) t[e] = (f32x2){__builtin_amdgcn_exp2f(t[e].x), __builtin_amdgcn_exp2f(t[e].y)};
#pragma unroll
      for (int e = 0; e < 8; ++e) t[e] = t[e] + 1.f;
      __builtin_amdgcn_sched_barrier(0);
      WLOAD(15 + 3 * a);
#pragma unroll
      for (int e = 0; e < 8; ++e) t[e] = (f32x2){__builtin_amdgcn_rcpf(t[e].x), __builtin_amdgcn_rcpf(t[e].y)};
      __builtin_amdgcn_sched_barrier(0);
      WLOAD(16 + 3 * a);
      unsigned o[8];
#pragma unroll
      for (int e = 0; e < 8; ++e) { f32x2 g = v[e] * t[e]; o[e] = pack2(g.x, g.y); }
      const int fstep = (f0 + fl) >> 4;
#pragma unroll
      for (int s = 0; s < 2; ++s) {
        u32x4 ov = {o[4 * s], o[4 * s + 1], o[4 * s + 2], o[4 * s + 3]};
        asm volatile("global_store_dwordx4 %0, %1, off sc0 sc1\n\ts_nop 1" ::"v"(Hf + (((size_t)ltile * (FF / 16) + fstep + s) * 64 + lane) * 8), "v"(ov) : "memory");
      }
    }
  }
  STAMP(2, 3);
  asm volatile("s_waitcnt vmcnt(0) lgkmcnt(0)\n\ts_barrier" ::: "memory");
  unsigned short hres[8];
  u32x2 bres[2];
#pragma unroll
  for (int e = 0; e < 8; ++e) hres[e] = hT[(size_t)(dbase + (e & 3) + 8 * (e >> 2)) * LL + lrow];
#pragma unroll
  for (int g = 0; g < 2; ++g) bres[g] = *(const u32x2*)(b2 + dbase + 8 * g);
  asm volatile("" ::: "memory");
  if (tid == 0) {
    unsigned* fl = flags + lt * 64;
    __hip_atomic_fetch_add(fl, 1u, __ATOMIC_RELAXED, __HIP_MEMORY_SCOPE_AGENT);
    unsigned spins = 0;
    while (__hip_atomic_load(fl, __ATOMIC_RELAXED, __HIP_MEMORY_SCOPE_AGENT) < 14u) {
      __builtin_amdgcn_s_sleep(1);
      if (++spins > (1u << 24)) break;
    }
  }
  asm volatile("s_waitcnt lgkmcnt(0)\n\ts_barrier" ::: "memory");
  STAMP(2, 4);
  {
    const unsigned short* hsrc2 = Hf + (((size_t)(lt2 * 2) * (FF / 16) + wv) * 64 + lane) * 8;
    auto stage2 = [&](int c) {
      char* base = smem + (c & 7) * 16384;
#pragma unroll
      for (int i = 0; i < 2; ++i)
        __builtin_amdgcn_global_load_lds((const unsigned*)(hsrc2 + ((size_t)i * (FF / 16) + c * 8) * 512),
                                         (unsigned*)(base + (i * 8 + wv) * 1024), 16, 0, 16);
    };
#pragma unroll
    for (int c = 0; c < 5; ++c) stage2(c);
    f32x16 ac2[2];
#pragma unroll
    for (int b = 0; b < 2; ++b)
#pragma unroll
      for (int e = 0; e < 16; ++e) ac2[b][e] = 0.f;
    const unsigned aoff = lb + kq * 1024 + lane * 16;
    u32x4 Hr[2][2][2];
    WAIT_V_BAR(8);
    STAMP(2, 5);
#pragma unroll
    for (int j = 0; j < 2; ++j)
#pragma unroll
      for (int b = 0; b < 2; ++b) DS_B128(Hr[0][j][b], aoff, j * 4096 + b * 8192);
#pragma unroll
    for (int c = 0; c < 14; ++c) {
      const int set = c & 1;
      if (c < 13) {
        if (c == 0) { WAIT_V_BAR(6); } else if (c == 1) { WAIT_V_BAR(7); } else if (c == 2) { WAIT_V_BAR(8); }
        else if (c == 3) { WAIT_V_BAR(9); } else if (c < 9) { WAIT_V_BAR(10); } else if (c == 9) { WAIT_V_BAR(9); }
        else if (c == 10) { WAIT_V_BAR(6); } else if (c == 11) { WAIT_V_BAR(3); } else { WAIT_V_BAR(0); }
        if (c + 5 < 14) stage2(c + 5);
        if (c < 8) WLOAD(20 + c);
      } else {
        WAIT_L0();
      }
      asm volatile("" : "+v"(Hr[set][0][0]), "+v"(Hr[set][0][1]), "+v"(Hr[set][1][0]), "+v"(Hr[set][1][1]));
      if (c == 0) {
#pragma unroll
        for (int i = 0; i < 20; i += 4) asm volatile("" : "+v"(Wf[i]), "+v"(Wf[i + 1]), "+v"(Wf[i + 2]), "+v"(Wf[i + 3]));
      }
      if (c == 10) asm volatile("" : "+v"(Wf[20]), "+v"(Wf[21]), "+v"(Wf[22]), "+v"(Wf[23]), "+v"(Wf[24]), "+v"(Wf[25]));
      if (c == 13) asm volatile("" : "+v"(Wf[26]), "+v"(Wf[27]));
      const unsigned aa = aoff + ((c + 1) & 7) * 16384;
      __builtin_amdgcn_sched_barrier(0);
#pragma unroll
      for (int j = 0; j < 2; ++j)
#pragma unroll
        for (int b = 0; b < 2; ++b) {
          ac2[b] = __builtin_amdgcn_mfma_f32_32x32x16_bf16(__builtin_bit_cast(bf16x8, Wf[2 * c + j]),
                                                           __builtin_bit_cast(bf16x8, Hr[set][j][b]), ac2[b], 0, 0, 0);
          if (c < 13) DS_B128(Hr[set ^ 1][j][b], aa, j * 4096 + b * 8192);
          __builtin_amdgcn_sched_barrier(0);
        }
    }
    STAMP(2, 6);
    RAW_BAR();
    {
      f32x4* xb = (f32x4*)(smem + wv * 8192) + lane;
#pragma unroll
      for (int Q = 0; Q < 4; ++Q)
#pragma unroll
        for (int g = 0; g < 2; ++g) {
          const int r = 8 * (Q & 1) + 4 * g;
          xb[(Q * 2 + g) * 64] = (f32x4){ac2[Q >> 1][r], ac2[Q >> 1][r + 1], ac2[Q >> 1][r + 2], ac2[Q >> 1][r + 3]};
        }
    }
    RAW_BAR();
#pragma unroll
    for (int g = 0; g < 2; ++g) {
      f32x4 t = {0.f, 0.f, 0.f, 0.f};
#pragma unroll
      for (int k2 = 0; k2 < 4; ++k2) t += *((const f32x4*)(smem + (k2 * 2 + wd2) * 8192) + (kq * 2 + g) * 64 + lane);
      float bv[4] = {bflo(bres[g][0]), bfhi(bres[g][0]), bflo(bres[g][1]), bfhi(bres[g][1])};
      float o[4];
#pragma unroll
      for (int e = 0; e < 4; ++e) o[e] = bf2f(hres[4 * g + e]) + (t[e] + bv[e]);
      u32x2 ov = {pack2(o[0], o[1]), pack2(o[2], o[3])};
      *(u32x2*)(out + (size_t)lrow * DD + dbase + 8 * g) = ov;
    }
  }
  STAMP(2, 7);
}

extern "C" void kernel_launch(void* const* d_in, const int* in_sizes, int n_in, void* d_out,
                              int out_size, void* d_ws, size_t ws_size, hipStream_t stream) {
  const unsigned short* x = (const unsigned short*)d_in[0];
  const unsigned short* logA = (const unsigned short*)d_in[1];
  const unsigned short* Aim = (const unsigned short*)d_in[2];
  const unsigned short* Cre = (const unsigned short*)d_in[3];
  const unsigned short* Cim = (const unsigned short*)d_in[4];
  const unsigned short* logdt = (const unsigned short*)d_in[5];
  const unsigned short* Dskip = (const unsigned short*)d_in[6];
  const unsigned short* w1 = (const unsigned short*)d_in[8];
  const unsigned short* b1 = (const unsigned short*)d_in[9];
  const unsigned short* w2 = (const unsigned short*)d_in[10];
  const unsigned short* b2 = (const unsigned short*)d_in[11];
  unsigned short* out = (unsigned short*)d_out;

  char* ws = (char*)d_ws;
  const size_t T = (size_t)DD * LL * 2;
  unsigned short* uT = (unsigned short*)(ws);
  unsigned short* xT = (unsigned short*)(ws + T);
  unsigned short* Kx = (unsigned short*)(ws + 2 * T);
  unsigned short* hT = (unsigned short*)(ws + 3 * T);
  float* S1 = (float*)(ws + 4 * T);
  unsigned short* Hf = (unsigned short*)(ws + 4 * T + 8192);
  u32x4* W2f = (u32x4*)(ws + 4 * T + 8192 + (size_t)LL * FF * 2);
  unsigned* flags = (unsigned*)(ws + 4 * T + 8192 + (size_t)LL * FF * 2 + (size_t)FF * DD * 2);

  (void)hipFuncSetAttribute((const void*)k34_kernel, hipFuncAttributeMaxDynamicSharedMemorySize, K3_LDS);

  k1_kernel<<<K1_KG_BLOCKS + K1_LN_BLOCKS, 256, 0, stream>>>(x, logA, Aim, Cre, Cim, logdt, w1, w2, uT, xT, Kx, flags);
  k2_kernel<<<DD + K2_AUX, 256, 0, stream>>>(uT, xT, Kx, Dskip, w1, w2, hT, S1, W2f);
  k34_kernel<<<224, 512, K3_LDS, stream>>>(hT, w1, S1, b1, W2f, b2, Hf, flags, out);
}
```

```cpp
#include <hip/hip_runtime.h>
#include <hip/hip_bf16.h>
#include <stdint.h>

#define LL 2048
#define DD 448
#define NN 256
#define FF 1792

typedef __bf16 bf16x2 __attribute__((ext_vector_type(2)));
typedef __bf16 bf16x8 __attribute__((ext_vector_type(8)));
typedef _Float16 f16x2 __attribute__((ext_vector_type(2)));
typedef _Float16 f16x8 __attribute__((ext_vector_type(8)));
typedef float f32x2 __attribute__((ext_vector_type(2)));
typedef float f32x4 __attribute__((ext_vector_type(4)));
typedef float f32x16 __attribute__((ext_vector_type(16)));
typedef unsigned u32x4 __attribute__((ext_vector_type(4)));
typedef unsigned u32x2 __attribute__((ext_vector_type(2)));

#define DI __device__ __forceinline__

#define STAMP(k, i)

DI float bfr(float x) { return (float)(__bf16)x; }
DI float bf2f(unsigned short v) { return __uint_as_float(((unsigned)v) << 16); }
DI float bflo(unsigned w) { return __uint_as_float(w << 16); }
DI float bfhi(unsigned w) { return __uint_as_float(w & 0xffff0000u); }
DI unsigned pack2(float a, float b) {
  bf16x2 r = {(__bf16)a, (__bf16)b};
  return __builtin_bit_cast(unsigned, r);
}
DI unsigned packh(f32x2 a) {
  f16x2 r = {(_Float16)a.x, (_Float16)a.y};
  return __builtin_bit_cast(unsigned, r);
}
DI unsigned lds_addr(const void* p) {
  return (unsigned)(uintptr_t)(__attribute__((address_space(3))) const char*)p;
}

#define WAIT_V_BAR(n) asm volatile("s_waitcnt vmcnt(" #n ") lgkmcnt(0)\n\ts_barrier" ::: "memory")
#define WAIT_L0() asm volatile("s_waitcnt lgkmcnt(0)" ::: "memory")
#define DS_TR(dst, addr, off) asm volatile("ds_read_b64_tr_b16 %0, %1 offset:%2" : "=v"(dst) : "v"(addr), "i"(off))
#define DS_B128(dst, addr, off) asm volatile("ds_read_b128 %0, %1 offset:%2" : "=v"(dst) : "v"(addr), "i"(off))

DI void st_wt16(void* p, u32x4 v) { asm volatile("global_store_dwordx4 %0, %1, off sc0 sc1\n\ts_nop 1" ::"v"(p), "v"(v) : "memory"); }
DI void st_wt8(void* p, u32x2 v) { asm volatile("global_store_dwordx2 %0, %1, off sc0 sc1\n\ts_nop 1" ::"v"(p), "v"(v) : "memory"); }

DI bf16x8 cat_frag(u32x2 lo, u32x2 hi) {
  u32x4 v = __builtin_shufflevector(lo, hi, 0, 1, 2, 3);
  return __builtin_bit_cast(bf16x8, v);
}

#define K1_LN_BLOCKS 64
#define K1_KG_BLOCKS DD
#define K1_ROWB 272
#define K1_WREG (48 * K1_ROWB)
#define K1_LNROW 928

DI f32x2 cexp_p(float lr2, float lih, float lil, float p) {
  float mag = __builtin_amdgcn_exp2f(lr2 * p);
  float ph = lih * p;
  float e = __builtin_fmaf(lih, p, -ph);
  float fr = __builtin_amdgcn_fractf(ph) + __builtin_fmaf(lil, p, e);
  f32x2 cs = {__builtin_amdgcn_cosf(fr), __builtin_amdgcn_sinf(fr)};
  return cs * mag;
}
DI f32x2 crot(f32x2 w) { return (f32x2){-w.y, w.x}; }
DI f32x2 cmul(f32x2 a, f32x2 w, f32x2 wr) { return __builtin_elementwise_fma(a.yy, wr, a.xx * w); }
DI int cidx_of_lag(int lag) {
  __bf16 b = (__bf16)(float)lag;
  int bits = (int)__builtin_bit_cast(unsigned short, b);
  return lag < 128 ? lag : bits - (0x4300 - 128);
}
DI float sum8(float v) {
  v += __builtin_bit_cast(float, __builtin_amdgcn_mov_dpp(__builtin_bit_cast(int, v), 0xB1, 0xF, 0xF, true));
  v += __builtin_bit_cast(float, __builtin_amdgcn_mov_dpp(__builtin_bit_cast(int, v), 0x4E, 0xF, 0xF, true));
  v += __builtin_bit_cast(float, __builtin_amdgcn_mov_dpp(__builtin_bit_cast(int, v), 0x141, 0xF, 0xF, true));
  return v;
}
typedef short s16x4 __attribute__((ext_vector_type(4)));
typedef __attribute__((address_space(3))) s16x4* lds_s16x4_ptr;

__global__ __launch_bounds__(256) void k1_kernel(
    const unsigned short* __restrict__ x, const unsigned short* __restrict__ logA,
    const unsigned short* __restrict__ Aim, const unsigned short* __restrict__ Cre,
    const unsigned short* __restrict__ Cim, const unsigned short* __restrict__ logdt,
    const unsigned short* __restrict__ w1, const unsigned short* __restrict__ w2,
    unsigned short* __restrict__ uT, unsigned short* __restrict__ xT,
    unsigned short* __restrict__ Kx, unsigned* __restrict__ flags) {
  __shared__ __attribute__((aligned(16))) char smem[4 * K1_WREG];
  const int tid = threadIdx.x, lane = tid & 63, wv = __builtin_amdgcn_readfirstlane(tid >> 6);
  const int bid = blockIdx.x;
  STAMP(0, 0);
  if (bid < 12) flags[bid * 256 + tid] = 0u;
  if (bid >= K1_KG_BLOCKS) {
    __builtin_amdgcn_s_setprio(3);
    const int l0 = (bid - K1_KG_BLOCKS) * 32;
    const int r = lane >> 3, c = lane & 7;
    const int row = wv * 8 + r;
    u32x4 raw[7];
#pragma unroll
    for (int i = 0; i < 7; ++i) raw[i] = *(const u32x4*)(x + (size_t)(l0 + row) * DD + (c + 8 * i) * 8);
    f32x2 s2 = {0.f, 0.f};
#pragma unroll
    for (int i = 0; i < 7; ++i)
#pragma unroll
      for (int e = 0; e < 4; ++e) s2 += (f32x2){bflo(raw[i][e]), bfhi(raw[i][e])};
    const float mean = sum8(s2.x + s2.y) * (1.f / DD);
    f32x2 q2 = {0.f, 0.f};
    const f32x2 mm = {mean, mean};
#pragma unroll
    for (int i = 0; i < 7; ++i)
#pragma unroll
      for (int e = 0; e < 4; ++e) { f32x2 t = (f32x2){bflo(raw[i][e]), bfhi(raw[i][e])} - mm; q2 = __builtin_elementwise_fma(t, t, q2); }
    const float var = sum8(q2.x + q2.y) * (1.f / DD);
    const float m = bfr(mean);
    const float sd = bfr(sqrtf(bfr(bfr(var) + 1e-5f)));
    const float rs = 1.f / sd;
    const f32x2 m2 = {m, m}, rs2 = {rs, rs}, nsd2 = {-sd, -sd};
    char* tu = smem;
#pragma unroll
    for (int i = 0; i < 7; ++i) {
      u32x4 o;
#pragma unroll
      for (int e = 0; e < 4; ++e) {
        f32x2 t = (f32x2){bflo(raw[i][e]), bfhi(raw[i][e])} - m2;
        unsigned tb = pack2(t.x, t.y);
        f32x2 dd = {bflo(tb), bfhi(tb)};
        f32x2 qv = dd * rs2;
        f32x2 er = __builtin_elementwise_fma(qv, nsd2, dd);
        qv = __builtin_elementwise_fma(er, rs2, qv);
        o[e] = pack2(qv.x, qv.y);
      }
      *(u32x4*)(tu + row * K1_LNROW + (c + 8 * i) * 16) = o;
    }
    __syncthreads();
    const int g = lane >> 4, q = (lane & 15) >> 2, pp = lane & 3;
    const char* rb = tu + (8 * g + q) * K1_LNROW + pp * 8;
#pragma unroll
    for (int it = 0; it < 7; ++it) {
      const int c0 = (it * 4 + wv) * 16;
      s16x4 a = __builtin_amdgcn_ds_read_tr16_b64_v4i16((lds_s16x4_ptr)(rb + c0 * 2));
      s16x4 b = __builtin_amdgcn_ds_read_tr16_b64_v4i16((lds_s16x4_ptr)(rb + 4 * K1_LNROW + c0 * 2));
      u32x2 a2 = __builtin_bit_cast(u32x2, a), b2 = __builtin_bit_cast(u32x2, b);
      u32x4 o = {a2[0], a2[1], b2[0], b2[1]};
      st_wt16(uT + (size_t)(c0 + (lane & 15)) * LL + l0 + 8 * g, o);
    }
    __syncthreads();
#pragma unroll
    for (int i = 0; i < 7; ++i) *(u32x4*)(tu + row * K1_LNROW + (c + 8 * i) * 16) = raw[i];
    __syncthreads();
#pragma unroll
    for (int it = 0; it < 7; ++it) {
      const int c0 = (it * 4 + wv) * 16;
      s16x4 a = __builtin_amdgcn_ds_read_tr16_b64_v4i16((lds_s16x4_ptr)(rb + c0 * 2));
      s16x4 b = __builtin_amdgcn_ds_read_tr16_b64_v4i16((lds_s16x4_ptr)(rb + 4 * K1_LNROW + c0 * 2));
      u32x2 a2 = __builtin_bit_cast(u32x2, a), b2 = __builtin_bit_cast(u32x2, b);
      u32x4 o = {a2[0], a2[1], b2[0], b2[1]};
      st_wt16(xT + (size_t)(c0 + (lane & 15)) * LL + l0 + 8 * g, o);
    }
  } else {
    const int d = bid;
    const int nb = d * NN + tid;
    float lr2, lih, lil, cr, ci;
    u32x4 pf1, pf2;
    {
      const unsigned short la = logA[nb], ai = Aim[nb], cre = Cre[nb], cim = Cim[nb];
      const unsigned short ldt = logdt[d];
      const float dt = bfr(expf(bf2f(ldt)));
      const float I2PH = 0.15915494f;
      const float I2PL = 6.4206382e-09f;
      const float lr = -bfr(expf(bf2f(la))) * dt;
      const float li = bf2f(ai) * dt;
      lr2 = lr * 1.44269504f;
      lih = li * I2PH;
      lil = __builtin_fmaf(li, I2PH, -lih) + li * I2PL;
      cr = 2.f * bf2f(cre) * dt;
      ci = 2.f * bf2f(cim) * dt;
    }
    float cm = fmaxf(fabsf(cr), fabsf(ci));
#pragma unroll
    for (int o = 32; o > 0; o >>= 1) cm = fmaxf(cm, __shfl_xor(cm, o));
    const float scale = cm > 0.f ? cm : 1.f;
    const float inv = 1.f / scale;
    STAMP(0, 1);
    {
      const int pi = d * 224 + (tid < 224 ? tid : 223);
      asm volatile("global_load_dwordx4 %0, %1, off" : "=v"(pf1) : "v"((const u32x4*)w1 + pi) : "memory");
      asm volatile("global_load_dwordx4 %0, %1, off" : "=v"(pf2) : "v"((const u32x4*)w2 + pi) : "memory");
    }
    char* reg = smem + wv * K1_WREG;
    unsigned* wr_ = (unsigned*)(reg + lane * 4);
#define KROW(r) wr_[(r) * (K1_ROWB / 4)]
    const int i16 = lane & 15, kg = lane >> 4;
    const char* rdA = reg + i16 * K1_ROWB + kg * 16;
    f32x4 acc0 = {0, 0, 0, 0}, acc1 = {0, 0, 0, 0}, acc2 = {0, 0, 0, 0};
    {
      const f32x2 E1 = cexp_p(lr2, lih, lil, 1.f), E8 = cexp_p(lr2, lih, lil, 8.f), E128 = cexp_p(lr2, lih, lil, 128.f);
      const f32x2 E256 = cexp_p(lr2, lih, lil, 256.f), E512 = cexp_p(lr2, lih, lil, 512.f), E1024 = cexp_p(lr2, lih, lil, 1024.f);
      const f32x2 E1r = crot(E1), E128r = crot(E128), E256r = crot(E256), E512r = crot(E512);
      f32x2 a[16];
      {
        const f32x2 w = {E8.x, -E8.y}, wr = crot(w);
        const f32x2 w2 = cmul(w, w, wr), w2r = crot(w2);
        const f32x2 w4 = cmul(w2, w2, w2r), w4r = crot(w4);
        a[0] = (f32x2){cr * inv, -ci * inv};
        a[1] = cmul(a[0], w, wr);
        a[2] = cmul(a[0], w2, w2r);
        a[3] = cmul(a[1], w2, w2r);
#pragma unroll
        for (int i = 4; i < 16; ++i) a[i] = cmul(a[i - 4], w4, w4r);
      }
      f32x2 e[8];
      e[0] = (f32x2){1.f, 0.f}; e[1] = E1;
      e[2] = cmul(E1, E1, E1r);
      e[3] = cmul(e[2], E1, E1r);
      const f32x2 e2r = crot(e[2]);
      e[4] = cmul(e[2], e[2], e2r);
      const f32x2 e4r = crot(e[4]);
      e[5] = cmul(e[1], e[4], e4r); e[6] = cmul(e[2], e[4], e4r); e[7] = cmul(e[3], e[4], e4r);
#pragma unroll
      for (int i = 0; i < 16; ++i) KROW(i) = packh(a[i]);
#pragma unroll
      for (int j = 0; j < 8; ++j) { KROW(16 + j) = packh(e[j]); KROW(24 + j) = packh(j ? cmul(e[j], E128, E128r) : E128); }
      const f32x2 E384 = cmul(E256, E128, E128r), E384r = crot(E384);
#pragma unroll
      for (int r = 0; r < 4; ++r) {
        KROW(32 + r) = packh(r ? cmul(e[2 * r], E256, E256r) : E256);
        KROW(36 + r) = packh(r ? cmul(e[2 * r], E384, E384r) : E384);
      }
      f32x2 u[4];
      u[0] = E512; u[1] = cmul(E512, E128, E128r); u[2] = cmul(E512, E256, E256r); u[3] = cmul(E512, E384, E384r);
#pragma unroll
      for (int w = 0; w < 4; ++w) { KROW(40 + 2 * w) = packh(u[w]); KROW(41 + 2 * w) = packh(cmul(u[w], e[4], e4r)); }
      f32x2 v[9];
      v[0] = E1024; v[1] = cmul(E1024, E128, E128r); v[2] = cmul(E1024, E256, E256r); v[3] = cmul(E1024, E384, E384r);
#pragma unroll
      for (int c = 4; c < 9; ++c) v[c] = cmul(v[c - 4], E512, E512r);
      unsigned vp[9];
#pragma unroll
      for (int c = 0; c < 9; ++c) vp[c] = packh(v[c]);
      __builtin_amdgcn_fence(__ATOMIC_RELEASE, "wavefront");
      __builtin_amdgcn_wave_barrier();
      __builtin_amdgcn_fence(__ATOMIC_ACQUIRE, "wavefront");
      f16x8 af[4];
#pragma unroll
      for (int s = 0; s < 4; ++s) {
        af[s] = *(const f16x8*)(rdA + 64 * s);
        f16x8 b0 = *(const f16x8*)(rdA + 16 * K1_ROWB + 64 * s);
        f16x8 b1 = *(const f16x8*)(rdA + 32 * K1_ROWB + 64 * s);
        acc0 = __builtin_amdgcn_mfma_f32_16x16x32_f16(af[s], b0, acc0, 0, 0, 0);
        acc1 = __builtin_amdgcn_mfma_f32_16x16x32_f16(af[s], b1, acc1, 0, 0, 0);
      }
      __builtin_amdgcn_fence(__ATOMIC_RELEASE, "wavefront");
      __builtin_amdgcn_wave_barrier();
      __builtin_amdgcn_fence(__ATOMIC_ACQUIRE, "wavefront");
#pragma unroll
      for (int c = 0; c < 9; ++c) KROW(16 + c) = vp[c];
      __builtin_amdgcn_fence(__ATOMIC_RELEASE, "wavefront");
      __builtin_amdgcn_wave_barrier();
      __builtin_amdgcn_fence(__ATOMIC_ACQUIRE, "wavefront");
#pragma unroll
      for (int s = 0; s < 4; ++s) {
        f16x8 b2 = *(const f16x8*)(rdA + 16 * K1_ROWB + 64 * s);
        acc2 = __builtin_amdgcn_mfma_f32_16x16x32_f16(af[s], b2, acc2, 0, 0, 0);
      }
    }
#undef KROW
    acc0 *= scale; acc1 *= scale; acc2 *= scale;
    __syncthreads();
    STAMP(0, 2);
    float* red = (float*)smem;
    unsigned short* kd = (unsigned short*)(smem + 12288);
    *(f32x4*)(red + ((wv * 3 + 0) * 64 + lane) * 4) = acc0;
    *(f32x4*)(red + ((wv * 3 + 1) * 64 + lane) * 4) = acc1;
    *(f32x4*)(red + ((wv * 3 + 2) * 64 + lane) * 4) = acc2;
    __syncthreads();
    if (wv < 3) {
      f32x4 t = *(const f32x4*)(red + ((0 * 3 + wv) * 64 + lane) * 4);
#pragma unroll
      for (int w = 1; w < 4; ++w) t += *(const f32x4*)(red + ((w * 3 + wv) * 64 + lane) * 4);
      const int j = i16;
      const int lo0 = 128 * (j >> 3) + (j & 7);
      const int lo1 = j < 8 ? 256 + 128 * (j >> 2) + 2 * (j & 3) : 512 + 128 * ((j - 8) >> 1) + 4 * ((j - 8) & 1);
      const int lo2 = 1024 + 128 * j;
      const int lo = wv == 0 ? lo0 : (wv == 1 ? lo1 : lo2);
#pragma unroll
      for (int r = 0; r < 4; ++r) {
        const int pos = 8 * (4 * kg + r) + lo;
        __bf16 v0 = (__bf16)t[r];
        if (pos <= 2048) kd[cidx_of_lag(pos)] = __builtin_bit_cast(unsigned short, v0);
      }
    }
    __syncthreads();
    {
      unsigned o[4];
#pragma unroll
      for (int e = 0; e < 4; ++e) {
        const int lag = 2047 - (8 * tid + 2 * e);
        unsigned a = kd[cidx_of_lag(lag)], b = kd[cidx_of_lag(lag - 1)];
        o[e] = a | (b << 16);
      }
      u32x4 o0 = {o[0], o[1], o[2], o[3]};
      st_wt16(Kx + (size_t)d * LL + 8 * tid, o0);
    }
    asm volatile("" ::"v"(pf1), "v"(pf2));
  }
  STAMP(0, 3);
}

#define K2_CSTRIDE 4160
#define K2_CS_BLOCKS 28
#define K2_PK_BLOCKS 56
#define K2_AUX (K2_CS_BLOCKS + K2_PK_BLOCKS)
__global__ __launch_bounds__(256) void k2_kernel(
    const unsigned short* __restrict__ uT, const unsigned short* __restrict__ xT,
    const unsigned short* __restrict__ Kx, const unsigned short* __restrict__ Dskip,
    const unsigned short* __restrict__ w1, const unsigned short* __restrict__ w2,
    unsigned short* __restrict__ hT, float* __restrict__ S1, u32x4* __restrict__ W2f) {
  __shared__ __attribute__((aligned(16))) char smem[8 * K2_CSTRIDE + 4608];
  char* Kc = smem;
  char* Ul = smem + 8 * K2_CSTRIDE;
  const int tid = threadIdx.x, lane = tid & 63, wv = __builtin_amdgcn_readfirstlane(tid >> 6);
  STAMP(1, 0);
  const int abid = (int)blockIdx.x - DD;
  if (abid >= K2_PK_BLOCKS) {
    const int f0 = (abid - K2_PK_BLOCKS) * 64;
    float* red = (float*)smem;
    const int fc = tid & 7, rg = tid >> 3;
    u32x4 raw[14];
#pragma unroll
    for (int r = 0; r < 14; ++r) raw[r] = *(const u32x4*)(w1 + (size_t)(rg * 14 + r) * FF + f0 + fc * 8);
    f32x2 s[4] = {{0.f, 0.f}, {0.f, 0.f}, {0.f, 0.f}, {0.f, 0.f}};
#pragma unroll
    for (int r = 0; r < 14; ++r)
#pragma unroll
      for (int e = 0; e < 4; ++e) s[e] += (f32x2){bflo(raw[r][e]), bfhi(raw[r][e])};
    *(f32x4*)(red + rg * 64 + fc * 8) = (f32x4){s[0].x, s[0].y, s[1].x, s[1].y};
    *(f32x4*)(red + rg * 64 + fc * 8 + 4) = (f32x4){s[2].x, s[2].y, s[3].x, s[3].y};
    __syncthreads();
    if (tid < 64) {
      float t = 0.f;
#pragma unroll
      for (int r = 0; r < 32; ++r) t += red[r * 64 + tid];
      S1[f0 + tid] = t;
    }
    STAMP(1, 3);
    return;
  }
  if (abid >= 0) {
    const int f0 = abid * 32;
    u32x4 raw[7];
#pragma unroll
    for (int i = 0; i < 7; ++i) { const int e = tid + 256 * i; raw[i] = *(const u32x4*)(w2 + (size_t)(f0 + e / 56) * DD + (e % 56) * 8); }
#pragma unroll
    for (int i = 0; i < 7; ++i) { const int e = tid + 256 * i; *(u32x4*)(smem + (e / 56) * K1_LNROW + (e % 56) * 16) = raw[i]; }
    __syncthreads();
    const int hh = lane >> 5, blk = (lane >> 4) & 1, q = (lane & 15) >> 2, pp = lane & 3;
#pragma unroll
    for (int i = 0; i < 7; ++i) {
      const int fr = i * 4 + wv;
      const int dg = fr >> 1, sl = fr & 1;
      const char* rb = smem + (16 * sl + 4 * hh + q) * K1_LNROW + (dg * 32 + 16 * blk + 4 * pp) * 2;
      s16x4 a = __builtin_amdgcn_ds_read_tr16_b64_v4i16((lds_s16x4_ptr)rb);
      s16x4 b = __builtin_amdgcn_ds_read_tr16_b64_v4i16((lds_s16x4_ptr)(rb + 8 * K1_LNROW));
      u32x2 a2 = __builtin_bit_cast(u32x2, a), b2 = __builtin_bit_cast(u32x2, b);
      const int st = (f0 >> 4) + sl;
      st_wt16(W2f + ((size_t)(dg * 4 + (st & 3)) * 28 + (st >> 2)) * 64 + lane, (u32x4){a2[0], a2[1], b2[0], b2[1]});
    }
    STAMP(1, 3);
    return;
  }
  const int d = blockIdx.x;
  const int b = lane & 15, h = lane >> 4;
  u32x2 xx[2];
  float Dk;
  {
    const u32x4* kp = (const u32x4*)(Kx + (size_t)d * LL);
    u32x4 w0 = kp[tid];
    u32x4 w1 = {0, 0, 0, 0};
    if (tid < 255) w1 = kp[tid + 1];
    u32x4 uu = *(const u32x4*)(uT + (size_t)d * LL + tid * 8);
#pragma unroll
    for (int tt = 0; tt < 2; ++tt) xx[tt] = *(const u32x2*)(xT + (size_t)d * LL + 256 * (2 * wv + tt) + 16 * b + 4 * h);
    Dk = bf2f(Dskip[d]);
    unsigned w[8] = {w0[0], w0[1], w0[2], w0[3], w1[0], w1[1], w1[2], w1[3]};
#pragma unroll
    for (int m = 0; m < 4; ++m) {
      u32x4 ev = {w[m], w[m + 1], w[m + 2], w[m + 3]};
      u32x4 od;
#pragma unroll
      for (int i = 0; i < 4; ++i) od[i] = __builtin_amdgcn_alignbit(w[m + i + 1], w[m + i], 16);
      *(u32x4*)(Kc + (2 * m) * K2_CSTRIDE + tid * 16) = ev;
      *(u32x4*)(Kc + (2 * m + 1) * K2_CSTRIDE + tid * 16) = od;
    }
    u32x4 z = {0, 0, 0, 0};
    if (tid < 8) *(u32x4*)(Kc + tid * K2_CSTRIDE + 4096) = z;
    if (tid < 32) *(u32x4*)(Ul + tid * 16) = z;
    *(u32x4*)(Ul + 512 + tid * 16) = uu;
  }
  __syncthreads();
  STAMP(1, 1);
  f32x4 acc[8];
#pragma unroll
  for (int t = 0; t < 8; ++t) acc[t] = (f32x4){0, 0, 0, 0};
  const char* fbase = Kc + (7 - (b & 7)) * K2_CSTRIDE + 2 * (2040 - 8 * (b >> 3) + 8 * h);
  const char* gbase = Ul + 512 + 2 * (16 * b + 8 * h);
#pragma unroll
  for (int rr = 0; rr < 2; ++rr) {
    const int r = 4 * wv + 1 + 2 * rr;
    bf16x8 Fq[8], Gs[8];
#pragma unroll
    for (int q = 0; q < 8; ++q) Fq[q] = *(const bf16x8*)(fbase - 32 * (16 * q + r));
#pragma unroll
    for (int s = 0; s < 8; ++s) Gs[s] = *(const bf16x8*)(gbase + 32 * (16 * s - r));
#pragma unroll
    for (int q = 0; q < 8; ++q)
#pragma unroll
      for (int s = 0; s < 8 - q; ++s)
        acc[q + s] = __builtin_amdgcn_mfma_f32_16x16x32_bf16(Fq[q], Gs[s], acc[q + s], 0, 0, 0);
  }
  __syncthreads();
  STAMP(1, 2);
  float* red = (float*)smem;
#pragma unroll
  for (int t = 0; t < 8; ++t) *(f32x4*)(red + ((wv * 8 + t) * 64 + lane) * 4) = acc[t];
  __syncthreads();
#pragma unroll
  for (int tt = 0; tt < 2; ++tt) {
    const int t = 2 * wv + tt;
    f32x4 y = *(const f32x4*)(red + ((0 * 8 + t) * 64 + lane) * 4);
#pragma unroll
    for (int w = 1; w < 4; ++w) y += *(const f32x4*)(red + ((w * 8 + t) * 64 + lane) * 4);
    const int l = 256 * t + 16 * b + 4 * h;
    u32x2 uu = *(const u32x2*)(Ul + 512 + 2 * l);
    float u0 = bflo(uu[0]), u1 = bfhi(uu[0]), u2 = bflo(uu[1]), u3 = bfhi(uu[1]);
    float x0 = bflo(xx[tt][0]), x1 = bfhi(xx[tt][0]), x2 = bflo(xx[tt][1]), x3 = bfhi(xx[tt][1]);
    float h0 = x0 + bfr(bfr(y[0]) + bfr(u0 * Dk));
    float h1 = x1 + bfr(bfr(y[1]) + bfr(u1 * Dk));
    float h2 = x2 + bfr(bfr(y[2]) + bfr(u2 * Dk));
    float h3 = x3 + bfr(bfr(y[3]) + bfr(u3 * Dk));
    u32x2 o = {pack2(h0, h1), pack2(h2, h3)};
    st_wt8(hT + (size_t)d * LL + l, o);
  }
  STAMP(1, 3);
}

#define K34_STB 32
#define K3_NBUF 4
#define K3_CHUNK 32768
#define K3_XOFF (K3_NBUF * K3_CHUNK)
#define K3_LDS (K3_XOFF + 4096)
#define RAW_BAR() asm volatile("s_waitcnt lgkmcnt(0)\n\ts_barrier" ::: "memory")
__global__ __launch_bounds__(512) void k34_kernel(
    const unsigned short* __restrict__ hT, const unsigned short* __restrict__ w1,
    const float* __restrict__ S1, const unsigned short* __restrict__ b1,
    const u32x4* __restrict__ W2f, const unsigned short* __restrict__ b2,
    unsigned short* Hf, unsigned* flags, unsigned short* __restrict__ out) {
  extern __shared__ __attribute__((aligned(16))) char smem[];
  const int tid = threadIdx.x, lane = tid & 63, wv = __builtin_amdgcn_readfirstlane(tid >> 6);
  unsigned* stats2 = flags + 1024;
  if (blockIdx.x < K34_STB) {
    const int lq = tid & 7, dgp = tid >> 3;
    const int lb0 = blockIdx.x * 64;
    u32x4 raw[7];
#pragma unroll
    for (int i = 0; i < 7; ++i) raw[i] = *(const u32x4*)(hT + (size_t)(dgp * 7 + i) * LL + lb0 + lq * 8);
    f32x2 s1[4], s2[4];
#pragma unroll
    for (int e = 0; e < 4; ++e) { s1[e] = (f32x2){0.f, 0.f}; s2[e] = (f32x2){0.f, 0.f}; }
#pragma unroll
    for (int i = 0; i < 7; ++i)
#pragma unroll
      for (int e = 0; e < 4; ++e) { f32x2 t = {bflo(raw[i][e]), bfhi(raw[i][e])}; s1[e] += t; s2[e] = __builtin_elementwise_fma(t, t, s2[e]); }
    float* r1 = (float*)smem;
    float* r2 = r1 + 64 * 68;
#pragma unroll
    for (int e = 0; e < 4; ++e) { *(f32x2*)(r1 + dgp * 68 + lq * 8 + 2 * e) = s1[e]; *(f32x2*)(r2 + dgp * 68 + lq * 8 + 2 * e) = s2[e]; }
    __syncthreads();
    {
      const int row = tid & 63, part = tid >> 6;
      float a = 0.f, b = 0.f;
#pragma unroll
      for (int i = 0; i < 8; ++i) { a += r1[(part * 8 + i) * 68 + row]; b += r2[(part * 8 + i) * 68 + row]; }
      __syncthreads();
      r1[part * 68 + row] = a; r2[part * 68 + row] = b;
      __syncthreads();
      if (tid < 64) {
        float sa = 0.f, sb = 0.f;
#pragma unroll
        for (int i = 0; i < 8; ++i) { sa += r1[i * 68 + tid]; sb += r2[i * 68 + tid]; }
        const float mean = sa * (1.f / DD);
        const float var = fmaxf(sb * (1.f / DD) - mean * mean, 0.f);
        const float sd = sqrtf(bfr(bfr(var) + 1e-5f));
        __hip_atomic_store(stats2 + lb0 + tid, pack2(mean, sd), __ATOMIC_RELAXED, __HIP_MEMORY_SCOPE_AGENT);
      }
    }
    return;
  }
  const int bid = blockIdx.x - K34_STB;
  const int xcd = bid & 7, idx = bid >> 3;
  const int lt = 2 * xcd + (idx / 14), ft = idx % 14;
  const int l0 = lt * 128, f0 = ft * 128;
  const int wf = wv >> 2, wl = wv & 3;

  const int srow = lane >> 4, sch = lane & 15;
  const unsigned short* wsrc = w1 + (size_t)(wv * 4 + srow) * FF + f0 + (sch ^ ((srow & 3) << 2)) * 8;
  const unsigned short* hsrc = hT + (size_t)(wv * 4 + srow) * LL + l0 + (sch ^ ((srow & 3) << 2)) * 8;
  auto stage = [&](int c, int buf) {
    char* base = smem + buf * K3_CHUNK;
#pragma unroll
    for (int i = 0; i < 2; ++i) {
      const int k = c * 64 + i * 32;
      __builtin_amdgcn_global_load_lds((const unsigned*)(wsrc + (size_t)k * FF),
                                       (unsigned*)(base + (i * 8 + wv) * 1024), 16, 0, 0);
      __builtin_amdgcn_global_load_lds((const unsigned*)(hsrc + (size_t)k * LL),
                                       (unsigned*)(base + 16384 + (i * 8 + wv) * 1024), 16, 0, 0);
    }
  };
  STAMP(2, 0);
  const int lt2 = 4 * xcd + (idx / 7), dt2 = idx % 7;
  const int kq = wv >> 1, wd2 = wv & 1;
  const int lrow = lt2 * 64 + (kq >> 1) * 32 + (lane & 31);
  const int dbase = dt2 * 64 + wd2 * 32 + 4 * (lane >> 5) + 16 * (kq & 1);
  if (wv < 2) __builtin_amdgcn_global_load_lds((const unsigned*)(S1 + f0 + tid), (unsigned*)(smem + K3_XOFF + wv * 256), 4, 0, 0);
  else if (wv == 2) __builtin_amdgcn_global_load_lds((const unsigned*)(b1 + f0 + lane * 2), (unsigned*)(smem + K3_XOFF + 512), 4, 0, 0);
  stage(0, 0); stage(1, 1); stage(2, 2); stage(3, 3);

  f32x16 acc[2];
#pragma unroll
  for (int a = 0; a < 2; ++a)
#pragma unroll
    for (int e = 0; e < 16; ++e) acc[a][e] = 0.f;

  const int hh = lane >> 5, blk = (lane >> 4) & 1, q = (lane & 15) >> 2, p = lane & 3;
  const unsigned lb = lds_addr(smem);
  unsigned roff[3];
#pragma unroll
  for (int c = 0; c < 2; ++c)
    roff[c] = lb + (8 * hh + q) * 256 + (4 * ((2 * wf + c) ^ q) + 2 * blk + (p >> 1)) * 16 + 8 * (p & 1);
  roff[2] = lb + 16384 + (8 * hh + q) * 256 + (4 * (wl ^ q) + 2 * blk + (p >> 1)) * 16 + 8 * (p & 1);
  u32x2 Rr[2][2][3][2];
  u32x4 Wf[28];
  unsigned sw;
  const u32x4* wp = W2f + ((size_t)((dt2 * 2 + wd2) * 4 + kq) * 28) * 64 + lane;
#define WLOAD(i) asm volatile("global_load_dwordx4 %0, %1, off offset:%2" : "=v"(Wf[i]) : "v"(wp + ((i) & ~3) * 64), "i"(((i) & 3) * 1024) : "memory")
  WAIT_V_BAR(12);
  STAMP(2, 1);
#pragma unroll
  for (int ks = 0; ks < 2; ++ks)
#pragma unroll
    for (int o = 0; o < 3; ++o) { DS_TR(Rr[0][ks][o][0], roff[o], ks * 4096); DS_TR(Rr[0][ks][o][1], roff[o], ks * 4096 + 1024); }
#pragma unroll
  for (int h = 0; h < 14; ++h) {
    const int set = h & 1, c = h >> 1;
    if (h & 1) {
      if (c == 0) { WAIT_V_BAR(8); } else if (c == 1) { WAIT_V_BAR(10); } else if (c == 2) { WAIT_V_BAR(12); }
      else if (c == 3) { WAIT_V_BAR(14); } else if (c == 4) { WAIT_V_BAR(10); } else if (c == 5) { WAIT_V_BAR(6); }
      else { WAIT_L0(); }
      if (c + 4 < 7) stage(c + 4, c & 3);
      if (c == 5)
        asm volatile("global_load_dword %0, %1, off sc0 sc1" : "=v"(sw) : "v"(stats2 + l0 + wl * 32 + (lane & 31)) : "memory");
      WLOAD(2 * c); WLOAD(2 * c + 1);
    } else {
      WAIT_L0();
    }
#pragma unroll
    for (int ks = 0; ks < 2; ++ks)
      asm volatile("" : "+v"(Rr[set][ks][0][0]), "+v"(Rr[set][ks][0][1]), "+v"(Rr[set][ks][1][0]), "+v"(Rr[set][ks][1][1]),
                        "+v"(Rr[set][ks][2][0]), "+v"(Rr[set][ks][2][1]));
    unsigned ra[3];
#pragma unroll
    for (int o = 0; o < 3; ++o) ra[o] = roff[o] + (((h + 1) >> 1) & 3) * K3_CHUNK + ((h + 1) & 1) * 8192;
    __builtin_amdgcn_sched_barrier(0);
#pragma unroll
    for (int ks = 0; ks < 2; ++ks) {
      bf16x8 fr[3];
#pragma unroll
      for (int o = 0; o < 3; ++o) fr[o] = cat_frag(Rr[set][ks][o][0], Rr[set][ks][o][1]);
#pragma unroll
      for (int a = 0; a < 2; ++a) {
        acc[a] = __builtin_amdgcn_mfma_f32_32x32x16_bf16(fr[a], fr[2], acc[a], 0, 0, 0);
        if (h < 13) {
          DS_TR(Rr[set ^ 1][ks][a][0], ra[a], ks * 4096); DS_TR(Rr[set ^ 1][ks][a][1], ra[a], ks * 4096 + 1024);
          DS_TR(Rr[set ^ 1][ks][2][a], ra[2], ks * 4096 + a * 1024);
        }
        __builtin_amdgcn_sched_barrier(0);
      }
      __builtin_amdgcn_sched_barrier(0);
    }
  }
  STAMP(2, 2);
  float mB, rB;
  {
    asm volatile("s_waitcnt vmcnt(4)" : "+v"(sw) : : "memory");
    unsigned spins = 0;
    while (!__all(sw != 0u) && ++spins < (1u << 20)) {
      __builtin_amdgcn_s_sleep(1);
      sw = __hip_atomic_load(stats2 + l0 + wl * 32 + (lane & 31), __ATOMIC_RELAXED, __HIP_MEMORY_SCOPE_AGENT);
    }
    mB = bflo(sw);
    rB = 1.f / bfhi(sw);
  }
  const float* Ss = (const float*)(smem + K3_XOFF);
  const unsigned short* Bs = (const unsigned short*)(smem + K3_XOFF + 512);
  {
    const float c0 = -2.f * 0.7978845608028654f * 1.4426950408889634f;
    const float c1 = c0 * 0.044715f;
    const f32x2 nm = {-mB, -mB}, rr = {rB, rB};
    const int ltile = (l0 >> 5) + wl;
#pragma unroll
    for (int a = 0; a < 2; ++a) {
      const int fl = wf * 64 + a * 32;
      f32x2 v[8], t[8];
#pragma unroll
      for (int g = 0; g < 4; ++g) {
        f32x4 s4 = *(const f32x4*)(Ss + fl + 8 * g + 4 * hh);
        u32x2 b2v = *(const u32x2*)(Bs + fl + 8 * g + 4 * hh);
        f32x2 a0 = {acc[a][4 * g], acc[a][4 * g + 1]}, a1 = {acc[a][4 * g + 2], acc[a][4 * g + 3]};
        v[2 * g] = __builtin_elementwise_fma(__builtin_elementwise_fma(nm, (f32x2){s4[0], s4[1]}, a0), rr, (f32x2){bflo(b2v[0]), bfhi(b2v[0])});
        v[2 * g + 1] = __builtin_elementwise_fma(__builtin_elementwise_fma(nm, (f32x2){s4[2], s4[3]}, a1), rr, (f32x2){bflo(b2v[1]), bfhi(b2v[1])});
      }
#pragma unroll
      for (int e = 0; e < 8; ++e) t[e] = __builtin_elementwise_fma(v[e] * v[e], (f32x2){c1, c1}, (f32x2){c0, c0});
#pragma unroll
      for (int e = 0; e < 8; ++e) t[e] = v[e] * t[e];
      __builtin_amdgcn_sched_barrier(0);
      WLOAD(14 + 3 * a);
#pragma unroll
      for (int e = 0; e < 8; ++e) t[e] = (f32x2){__builtin_amdgcn_exp2f(t[e].x), __builtin_amdgcn_exp2f(t[e].y)};
#pragma unroll
      for (int e = 0; e < 8; ++e) t[e] = t[e] + 1.f;
      __builtin_amdgcn_sched_barrier(0);
      WLOAD(15 + 3 * a);
#pragma unroll
      for (int e = 0; e < 8; ++e) t[e] = (f32x2){__builtin_amdgcn_rcpf(t[e].x), __builtin_amdgcn_rcpf(t[e].y)};
      __builtin_amdgcn_sched_barrier(0);
      WLOAD(16 + 3 * a);
      unsigned o[8];
#pragma unroll
      for (int e = 0; e < 8; ++e) { f32x2 g = v[e] * t[e]; o[e] = pack2(g.x, g.y); }
      const int fstep = (f0 + fl) >> 4;
#pragma unroll
      for (int s = 0; s < 2; ++s) {
        u32x4 ov = {o[4 * s], o[4 * s + 1], o[4 * s + 2], o[4 * s + 3]};
        asm volatile("global_store_dwordx4 %0, %1, off sc0 sc1\n\ts_nop 1" ::"v"(Hf + (((size_t)ltile * (FF / 16) + fstep + s) * 64 + lane) * 8), "v"(ov) : "memory");
      }
    }
  }
  STAMP(2, 3);
  asm volatile("s_waitcnt vmcnt(0) lgkmcnt(0)\n\ts_barrier" ::: "memory");
  unsigned short hres[8];
  u32x2 bres[2];
#pragma unroll
  for (int e = 0; e < 8; ++e) hres[e] = hT[(size_t)(dbase + (e & 3) + 8 * (e >> 2)) * LL + lrow];
#pragma unroll
  for (int g = 0; g < 2; ++g) bres[g] = *(const u32x2*)(b2 + dbase + 8 * g);
  asm volatile("" ::: "memory");
  if (tid == 0) {
    unsigned* fl = flags + lt * 64;
    __hip_atomic_fetch_add(fl, 1u, __ATOMIC_RELAXED, __HIP_MEMORY_SCOPE_AGENT);
    unsigned spins = 0;
    while (__hip_atomic_load(fl, __ATOMIC_RELAXED, __HIP_MEMORY_SCOPE_AGENT) < 14u) {
      __builtin_amdgcn_s_sleep(1);
      if (++spins > (1u << 20)) break;
    }
  }
  asm volatile("s_waitcnt lgkmcnt(0)\n\ts_barrier" ::: "memory");
  STAMP(2, 4);
  {
    const unsigned short* hsrc2 = Hf + (((size_t)(lt2 * 2) * (FF / 16) + wv) * 64 + lane) * 8;
    auto stage2 = [&](int c) {
      char* base = smem + (c & 7) * 16384;
#pragma unroll
      for (int i = 0; i < 2; ++i)
        __builtin_amdgcn_global_load_lds((const unsigned*)(hsrc2 + ((size_t)i * (FF / 16) + c * 8) * 512),
                                         (unsigned*)(base + (i * 8 + wv) * 1024), 16, 0, 16);
    };
#pragma unroll
    for (int c = 0; c < 5; ++c) stage2(c);
    f32x16 ac2[2];
#pragma unroll
    for (int b = 0; b < 2; ++b)
#pragma unroll
      for (int e = 0; e < 16; ++e) ac2[b][e] = 0.f;
    const unsigned aoff = lb + kq * 1024 + lane * 16;
    u32x4 Hr[2][2][2];
    WAIT_V_BAR(8);
    STAMP(2, 5);
#pragma unroll
    for (int j = 0; j < 2; ++j)
#pragma unroll
      for (int b = 0; b < 2; ++b) DS_B128(Hr[0][j][b], aoff, j * 4096 + b * 8192);
#pragma unroll
    for (int c = 0; c < 14; ++c) {
      const int set = c & 1;
      if (c < 13) {
        if (c == 0) { WAIT_V_BAR(6); } else if (c == 1) { WAIT_V_BAR(7); } else if (c == 2) { WAIT_V_BAR(8); }
        else if (c == 3) { WAIT_V_BAR(9); } else if (c < 9) { WAIT_V_BAR(10); } else if (c == 9) { WAIT_V_BAR(9); }
        else if (c == 10) { WAIT_V_BAR(6); } else if (c == 11) { WAIT_V_BAR(3); } else { WAIT_V_BAR(0); }
        if (c + 5 < 14) stage2(c + 5);
        if (c < 8) WLOAD(20 + c);
      } else {
        WAIT_L0();
      }
      asm volatile("" : "+v"(Hr[set][0][0]), "+v"(Hr[set][0][1]), "+v"(Hr[set][1][0]), "+v"(Hr[set][1][1]));
      if (c == 0) {
#pragma unroll
        for (int i = 0; i < 20; i += 4) asm volatile("" : "+v"(Wf[i]), "+v"(Wf[i + 1]), "+v"(Wf[i + 2]), "+v"(Wf[i + 3]));
      }
      if (c == 10) asm volatile("" : "+v"(Wf[20]), "+v"(Wf[21]), "+v"(Wf[22]), "+v"(Wf[23]), "+v"(Wf[24]), "+v"(Wf[25]));
      if (c == 13) asm volatile("" : "+v"(Wf[26]), "+v"(Wf[27]));
      const unsigned aa = aoff + ((c + 1) & 7) * 16384;
      __builtin_amdgcn_sched_barrier(0);
#pragma unroll
      for (int j = 0; j < 2; ++j)
#pragma unroll
        for (int b = 0; b < 2; ++b) {
          ac2[b] = __builtin_amdgcn_mfma_f32_32x32x16_bf16(__builtin_bit_cast(bf16x8, Wf[2 * c + j]),
                                                           __builtin_bit_cast(bf16x8, Hr[set][j][b]), ac2[b], 0, 0, 0);
          if (c < 13) DS_B128(Hr[set ^ 1][j][b], aa, j * 4096 + b * 8192);
          __builtin_amdgcn_sched_barrier(0);
        }
    }
    STAMP(2, 6);
    RAW_BAR();
    {
      f32x4* xb = (f32x4*)(smem + wv * 8192) + lane;
#pragma unroll
      for (int Q = 0; Q < 4; ++Q)
#pragma unroll
        for (int g = 0; g < 2; ++g) {
          const int r = 8 * (Q & 1) + 4 * g;
          xb[(Q * 2 + g) * 64] = (f32x4){ac2[Q >> 1][r], ac2[Q >> 1][r + 1], ac2[Q >> 1][r + 2], ac2[Q >> 1][r + 3]};
        }
    }
    RAW_BAR();
#pragma unroll
    for (int g = 0; g < 2; ++g) {
      f32x4 t = {0.f, 0.f, 0.f, 0.f};
#pragma unroll
      for (int k2 = 0; k2 < 4; ++k2) t += *((const f32x4*)(smem + (k2 * 2 + wd2) * 8192) + (kq * 2 + g) * 64 + lane);
      float bv[4] = {bflo(bres[g][0]), bfhi(bres[g][0]), bflo(bres[g][1]), bfhi(bres[g][1])};
      float o[4];
#pragma unroll
      for (int e = 0; e < 4; ++e) o[e] = bf2f(hres[4 * g + e]) + (t[e] + bv[e]);
      u32x2 ov = {pack2(o[0], o[1]), pack2(o[2], o[3])};
      *(u32x2*)(out + (size_t)lrow * DD + dbase + 8 * g) = ov;
    }
  }
  STAMP(2, 7);
}

extern "C" void kernel_launch(void* const* d_in, const int* in_sizes, int n_in, void* d_out,
                              int out_size, void* d_ws, size_t ws_size, hipStream_t stream) {
  const unsigned short* x = (const unsigned short*)d_in[0];
  const unsigned short* logA = (const unsigned short*)d_in[1];
  const unsigned short* Aim = (const unsigned short*)d_in[2];
  const unsigned short* Cre = (const unsigned short*)d_in[3];
  const unsigned short* Cim = (const unsigned short*)d_in[4];
  const unsigned short* logdt = (const unsigned short*)d_in[5];
  const unsigned short* Dskip = (const unsigned short*)d_in[6];
  const unsigned short* w1 = (const unsigned short*)d_in[8];
  const unsigned short* b1 = (const unsigned short*)d_in[9];
  const unsigned short* w2 = (const unsigned short*)d_in[10];
  const unsigned short* b2 = (const unsigned short*)d_in[11];
  unsigned short* out = (unsigned short*)d_out;

  char* ws = (char*)d_ws;
  const size_t T = (size_t)DD * LL * 2;
  unsigned short* uT = (unsigned short*)(ws);
  unsigned short* xT = (unsigned short*)(ws + T);
  unsigned short* Kx = (unsigned short*)(ws + 2 * T);
  unsigned short* hT = (unsigned short*)(ws + 3 * T);
  float* S1 = (float*)(ws + 4 * T);
  unsigned short* Hf = (unsigned short*)(ws + 4 * T + 8192);
  u32x4* W2f = (u32x4*)(ws + 4 * T + 8192 + (size_t)LL * FF * 2);
  unsigned* flags = (unsigned*)(ws + 4 * T + 8192 + (size_t)LL * FF * 2 + (size_t)FF * DD * 2);

  (void)hipFuncSetAttribute((const void*)k34_kernel, hipFuncAttributeMaxDynamicSharedMemorySize, K3_LDS);

  k1_kernel<<<K1_KG_BLOCKS + K1_LN_BLOCKS, 256, 0, stream>>>(x, logA, Aim, Cre, Cim, logdt, w1, w2, uT, xT, Kx, flags);
  k2_kernel<<<DD + K2_AUX, 256, 0, stream>>>(uT, xT, Kx, Dskip, w1, w2, hT, S1, W2f);
  k34_kernel<<<224 + K34_STB, 512, K3_LDS, stream>>>(hT, w1, S1, b1, W2f, b2, Hf, flags, out);
}
```
